```python
import torch

def _softmax(x, axis=-1):
    return torch.softmax(x, dim=axis)
def _gelu(x):
    return torch.nn.functional.gelu(x, approximate="tanh")
def _silu(x):
    return torch.nn.functional.silu(x)
def _sigmoid(x):
    return torch.sigmoid(x)
def _layernorm(x, axis=-1, eps=1e-5):
    m = x.mean(dim=axis, keepdim=True)
    v = x.var(dim=axis, keepdim=True, unbiased=False)
    return (x - m) / torch.sqrt(v + eps)
def _scatter_add(tgt, idx, src, axis=0):
    return tgt.clone().index_add_(axis, idx, src)
def _segment_sum(src, seg, n):
    out = torch.zeros((n,) + src.shape[1:], dtype=src.dtype)
    return out.index_add_(0, seg, src)
def _pad(x, pw):
    flat = tuple(v for pair in reversed(pw) for v in pair)
    return torch.nn.functional.pad(x, flat)

kernel_name = 'standalone__mha_block_cached__near_ridge'

def setup_inputs():

    _w = (lambda a: (lambda t: t.to(torch.bfloat16) if t.is_floating_point() else t)(torch.as_tensor(a)))
    B, Sq, D = 16, 96, 640
    H, Sk, Dh = 32, 1024, 192
    return {
        "x": _w((lambda *s: torch.randn(*s, dtype=torch.bfloat16) * 0.8)(B, Sq, D)),
        "k_cache": _w((lambda *s: torch.randn(*s, dtype=torch.bfloat16) * 0.8)(B, H, Sk, Dh)),
        "v_cache": _w((lambda *s: torch.randn(*s, dtype=torch.bfloat16) * 0.05)(B, H, Sk, Dh)),
        "wq": _w((lambda *s: torch.randn(*s, dtype=torch.bfloat16) * 0.8)(D, H * Dh) * (D ** -0.5)),
        "wo": _w((lambda *s: torch.randn(*s, dtype=torch.bfloat16) * 0.05)(H * Dh, D) * ((H * Dh) ** -0.5)),
    }

def reference(x, k_cache, v_cache, wq, wo):
    B, Sq, D = x.shape
    H, Dh = 32, 192
    q = (lambda x, *d: x.permute(*d))((x @ wq).reshape(B, Sq, H, Dh), 0, 2, 1, 3)
    scores = torch.einsum("bhqd,bhkd->bhqk", q, k_cache) * (Dh ** -0.5)
    p = _softmax(scores, axis=-1)
    o = torch.einsum("bhqk,bhkd->bhqd", p, v_cache)
    return (lambda x, *d: x.permute(*d))(o, 0, 2, 1, 3).reshape(B, Sq, H * Dh) @ wo
```

```cpp
#include <hip/hip_runtime.h>
#include <hip/hip_bf16.h>
#include <stdint.h>

typedef __bf16 bf16x8 __attribute__((ext_vector_type(8)));
typedef __bf16 bf16x4 __attribute__((ext_vector_type(4)));
typedef __bf16 bf16x2 __attribute__((ext_vector_type(2)));
typedef float f32x4 __attribute__((ext_vector_type(4)));
typedef float f32x2 __attribute__((ext_vector_type(2)));
typedef unsigned u32x4 __attribute__((ext_vector_type(4)));
typedef unsigned u32x2 __attribute__((ext_vector_type(2)));
#define AS3 __attribute__((address_space(3)))
#define DI __device__ __forceinline__
#define SB() __builtin_amdgcn_sched_barrier(0)

constexpr int NB = 16, SQ = 96, DM = 640, NH = 32, SK = 1024, DH = 192;
constexpr unsigned SLOT = 12288u;
constexpr int NSLOT = 10;
constexpr unsigned QBUF = NSLOT * SLOT;
constexpr unsigned MISC = QBUF + 36864u;
constexpr unsigned LDS_BYTES = 163840u;
constexpr unsigned KV_BYTES = (unsigned)NB * NH * SK * DH * 2u;
constexpr unsigned WQ_BYTES = (unsigned)DM * NH * DH * 2u;
constexpr unsigned X_BYTES = (unsigned)NB * SQ * DM * 2u;
constexpr float SM_C = 0.07216878364870322f * 1.4426950408889634f;
constexpr float RESC_THR = 6.0f / SM_C;

DI void dma16(unsigned ldsaddr, unsigned voff, __amdgpu_buffer_rsrc_t srd, unsigned soff) {
  asm volatile("s_mov_b32 m0, %0\n\ts_nop 0\n\tbuffer_load_dwordx4 %1, %2, %3 offen lds" :: "s"(ldsaddr), "v"(voff), "s"(srd), "s"(soff) : "memory");
}
DI void dma16nt(unsigned ldsaddr, unsigned voff, __amdgpu_buffer_rsrc_t srd, unsigned soff) {
  asm volatile("s_mov_b32 m0, %0\n\ts_nop 0\n\tbuffer_load_dwordx4 %1, %2, %3 offen nt lds" :: "s"(ldsaddr), "v"(voff), "s"(srd), "s"(soff) : "memory");
}
template <int N> DI void waitvm() { asm volatile("s_waitcnt vmcnt(%0)" :: "i"(N) : "memory"); }
DI void wait_vm_slots(int n) {
  switch (n) {
    case 0: waitvm<0>(); break;   case 1: waitvm<3>(); break;   case 2: waitvm<6>(); break;
    case 3: waitvm<9>(); break;   case 4: waitvm<12>(); break;  case 5: waitvm<15>(); break;
    case 6: waitvm<18>(); break;  case 7: waitvm<21>(); break;  case 8: waitvm<24>(); break;
    default: waitvm<27>(); break;
  }
}
DI void bar() { asm volatile("s_waitcnt lgkmcnt(0)\n\ts_barrier" ::: "memory"); }
DI unsigned rfl(unsigned x) { return __builtin_amdgcn_readfirstlane(x); }
DI bf16x8 ld128(unsigned a) { return *(const AS3 bf16x8*)(size_t)a; }
DI f32x4 ldf4(unsigned a) { return *(const AS3 f32x4*)(size_t)a; }
DI void st128(unsigned a, bf16x8 v) { *(AS3 bf16x8*)(size_t)a = v; }
DI void stf4(unsigned a, f32x4 v) { *(AS3 f32x4*)(size_t)a = v; }
DI bf16x4 ldtr(unsigned a) { return __builtin_amdgcn_ds_read_tr16_b64_v4bf16((AS3 bf16x4*)(size_t)a); }
DI bf16x8 cat(bf16x4 lo, bf16x4 hi) { return __builtin_shufflevector(lo, hi, 0, 1, 2, 3, 4, 5, 6, 7); }
DI f32x4 mfma(bf16x8 a, bf16x8 b, f32x4 c) { return __builtin_amdgcn_mfma_f32_16x16x32_bf16(a, b, c, 0, 0, 0); }
DI bf16x8 pack8(f32x4 a, f32x4 b) {
  bf16x4 lo = __builtin_convertvector(a, bf16x4), hi = __builtin_convertvector(b, bf16x4);
  return cat(lo, hi);
}
DI float xmax_g(float v) {
  auto a = __builtin_amdgcn_permlane16_swap(__float_as_uint(v), __float_as_uint(v), false, false);
  v = fmaxf(__uint_as_float(a[0]), __uint_as_float(a[1]));
  auto b = __builtin_amdgcn_permlane32_swap(__float_as_uint(v), __float_as_uint(v), false, false);
  return fmaxf(__uint_as_float(b[0]), __uint_as_float(b[1]));
}
DI float xsum_g(float v) {
  auto a = __builtin_amdgcn_permlane16_swap(__float_as_uint(v), __float_as_uint(v), false, false);
  v = __uint_as_float(a[0]) + __uint_as_float(a[1]);
  auto b = __builtin_amdgcn_permlane32_swap(__float_as_uint(v), __float_as_uint(v), false, false);
  return __uint_as_float(b[0]) + __uint_as_float(b[1]);
}

#define STAMP(i) do {} while (0)
#define STAMP_DUMP() do {} while (0)
DI int stream_start(int T) { return T == 0 ? 0 : (T <= 11 ? 4 + 7 * (T - 1) : 74 + 4 * (T - 11)); }
constexpr int STREAM_TOTAL = 74 + 4 * 21;

__global__ __launch_bounds__(512, 2) void attn_kernel(const void* __restrict__ xg, const void* __restrict__ kg, const void* __restrict__ vg,
                                                      const void* __restrict__ wqg, void* __restrict__ obuf) {
  extern __shared__ __attribute__((aligned(16))) char lds_raw[];
  const unsigned ldsb = (unsigned)(size_t)(AS3 char*)lds_raw;
  const int tid = threadIdx.x, lane = tid & 63;
  const int w = __builtin_amdgcn_readfirstlane(tid >> 6);
  const int r = lane & 15, g = lane >> 4;
  const int bid = blockIdx.x, xcd = bid & 7, idx = bid >> 3;
  const int b = idx >> 1, h0 = 2 * (2 * xcd + (idx & 1));
  const __amdgpu_buffer_rsrc_t srdK = __builtin_amdgcn_make_buffer_rsrc((void*)kg, 0, KV_BYTES, 0x00020000);
  const __amdgpu_buffer_rsrc_t srdV = __builtin_amdgcn_make_buffer_rsrc((void*)vg, 0, KV_BYTES, 0x00020000);
  const __amdgpu_buffer_rsrc_t srdW = __builtin_amdgcn_make_buffer_rsrc((void*)wqg, 0, WQ_BYTES, 0x00020000);
  const __amdgpu_buffer_rsrc_t srdX = __builtin_amdgcn_make_buffer_rsrc((void*)xg, 0, X_BYTES, 0x00020000);
  const unsigned kvbase = (unsigned)(b * NH + h0) * (unsigned)(SK * DH * 2);
  const int lw = w & 3;
  STAMP(0);
  unsigned voK[3], voV[3], voW[3], voX[3];
#pragma unroll
  for (int i = 0; i < 3; ++i) {
    const unsigned p = 64u * (3u * lw + i) + lane;
    const unsigned row = p / 24u, c = p % 24u;
    voK[i] = row * 384u + ((c ^ ((row >> 1) & 7u)) << 4);
    voV[i] = row * 384u + ((c ^ (((row >> 1) & 3u) << 1)) << 4);
    voW[i] = row * 12288u + ((c ^ ((((row >> 1) & 1u) | (((row >> 3) & 1u) << 1)) << 1)) << 4);
    const unsigned rx = p >> 3, cx = p & 7u;
    voX[i] = rx * 1280u + ((cx ^ ((rx >> 1) & 7u)) << 4);
  }
  const unsigned ldsw = ldsb + 3072u * lw;
  auto issueK = [&](unsigned pos, unsigned goff) {
#pragma unroll
    for (int i = 0; i < 3; ++i) dma16nt(rfl(ldsw + pos + 1024u * i), voK[i], srdK, rfl(goff));
  };
  auto issueV = [&](unsigned pos, unsigned goff) {
#pragma unroll
    for (int i = 0; i < 3; ++i) dma16nt(rfl(ldsw + pos + 1024u * i), voV[i], srdV, rfl(goff));
  };
  auto issueW = [&](unsigned pos, unsigned goff) {
#pragma unroll
    for (int i = 0; i < 3; ++i) dma16(rfl(ldsw + pos + 1024u * i), voW[i], srdW, rfl(goff));
  };
  auto issueX = [&](unsigned pos, unsigned goff) {
#pragma unroll
    for (int i = 0; i < 3; ++i) dma16(rfl(ldsw + pos + 1024u * i), voX[i], srdX, rfl(goff));
  };

  if (w < 4) {
    const int rh = w & 1, kh = w >> 1;
    issueK(0 * SLOT, kvbase); issueK(1 * SLOT, kvbase + SLOT);
    issueV(2 * SLOT, kvbase); issueV(3 * SLOT, kvbase + SLOT);
    for (int c = 0; c <= 8; ++c) bar();
    issueK(4 * SLOT, kvbase + 24576u); issueK(5 * SLOT, kvbase + 24576u + SLOT); issueV(6 * SLOT, kvbase + 24576u);
    bar();
    STAMP(1);
    const unsigned fk = (r >> 1) & 7;
    const unsigned ka0 = ldsb + kh * SLOT + 384u * r + (((unsigned)g ^ fk) << 4);
    const unsigned ka1 = ldsb + kh * SLOT + 384u * r + (((4u + g) ^ fk) << 4);
    const unsigned qa = ldsb + QBUF + (unsigned)rh * 18432u + lane * 16u;
    const unsigned qq = r >> 2, pp = r & 3;
    const unsigned fv = (2u * (g & 1) + (qq >> 1)) << 1;
    unsigned va[4];
#pragma unroll
    for (int d = 0; d < 4; ++d) va[d] = ldsb + kh * SLOT + 384u * (4u * g + qq) + (((2u * d + (pp >> 1)) ^ fv) << 4) + 8u * (pp & 1);

    for (int pr = 0; pr < 2; ++pr) {
      f32x4 O[12][3];
#pragma unroll
      for (int d = 0; d < 12; ++d)
#pragma unroll
        for (int q = 0; q < 3; ++q) O[d][q] = f32x4{0.f, 0.f, 0.f, 0.f};
      float mrun[3] = {-INFINITY, -INFINITY, -INFINITY}, lsum[3] = {0.f, 0.f, 0.f};
      for (int t = 0; t < 16; ++t) {
        const int T = pr * 16 + t;
        const int st = stream_start(T);
        const unsigned posK = (unsigned)(st % NSLOT), posV = (unsigned)((st + 2) % NSLOT);
        const unsigned kpos = (unsigned)((st + kh) % NSLOT) * SLOT - kh * SLOT;
        const unsigned vpos = (unsigned)((st + 2 + kh) % NSLOT) * SLOT - kh * SLOT;
        (void)posK; (void)posV;
        if (T == 0) waitvm<15>(); else if (T == 1) waitvm<3>();
        bar();
        STAMP(2 + 3 * T);
        f32x4 S[2][3];
#pragma unroll
        for (int q = 0; q < 3; ++q) { S[0][q] = f32x4{0.f, 0.f, 0.f, 0.f}; S[1][q] = f32x4{0.f, 0.f, 0.f, 0.f}; }
        const unsigned k0 = ka0 + kpos, k1 = ka1 + kpos;
        {
          bf16x8 kf[3][2], qf[3][3];
          auto ldstep = [&](int s) {
            const unsigned kb = ((s & 1) ? k1 : k0) + 128u * (s >> 1);
            kf[s % 3][0] = ld128(kb); kf[s % 3][1] = ld128(kb + 6144u);
#pragma unroll
            for (int q = 0; q < 3; ++q) qf[s % 3][q] = ld128(qa + (unsigned)(q * 6 + s) * 1024u);
          };
          ldstep(0); ldstep(1);
#pragma unroll
          for (int s = 0; s < 6; ++s) {
            SB();
            if (s + 2 < 6) ldstep(s + 2);
            SB();
#pragma unroll
            for (int q = 0; q < 3; ++q) {
              S[0][q] = mfma(kf[s % 3][0], qf[s % 3][q], S[0][q]);
              S[1][q] = mfma(kf[s % 3][1], qf[s % 3][q], S[1][q]);
            }
          }
          SB();
        }
        float mx[3];
        bool need = false;
#pragma unroll
        for (int q = 0; q < 3; ++q) {
          float m = fmaxf(fmaxf(fmaxf(S[0][q][0], S[0][q][1]), fmaxf(S[0][q][2], S[0][q][3])),
                          fmaxf(fmaxf(S[1][q][0], S[1][q][1]), fmaxf(S[1][q][2], S[1][q][3])));
          mx[q] = xmax_g(m);
          need |= (mx[q] > mrun[q] + RESC_THR);
        }
        if (__any(need)) {
#pragma unroll
          for (int q = 0; q < 3; ++q) {
            const float mn = fmaxf(mrun[q], mx[q]);
            const float al = __builtin_amdgcn_exp2f((mrun[q] - mn) * SM_C);
            mrun[q] = mn;
            lsum[q] *= al;
#pragma unroll
            for (int d = 0; d < 12; ++d) O[d][q] *= al;
          }
        }
        bf16x8 pf[3];
#pragma unroll
        for (int q = 0; q < 3; ++q) {
          const float nm = -mrun[q] * SM_C;
          f32x4 p0, p1;
#pragma unroll
          for (int i = 0; i < 4; ++i) {
            p0[i] = __builtin_amdgcn_exp2f(fmaf(S[0][q][i], SM_C, nm));
            p1[i] = __builtin_amdgcn_exp2f(fmaf(S[1][q][i], SM_C, nm));
          }
          lsum[q] += ((p0[0] + p0[1]) + (p0[2] + p0[3])) + ((p1[0] + p1[1]) + (p1[2] + p1[3]));
          pf[q] = pack8(p0, p1);
        }
        STAMP(3 + 3 * T);
        if (T == 0) waitvm<9>(); else if (T == 1) waitvm<0>();
        bar();
        STAMP(4 + 3 * T);
        {
          bf16x8 vf[4];
          auto ldv = [&](int d) {
            const unsigned a = va[d & 3] + vpos + 128u * (d >> 2);
            vf[d & 3] = cat(ldtr(a), ldtr(a + 6144u));
          };
          ldv(0); ldv(1); ldv(2);
#pragma unroll
          for (int d = 0; d < 12; ++d) {
            SB();
            if (d + 3 < 12) ldv(d + 3);
            SB();
#pragma unroll
            for (int q = 0; q < 3; ++q) O[d][q] = mfma(vf[d & 3], pf[q], O[d][q]);
          }
          SB();
        }
      }
      {
        const unsigned sa = ldsb + MISC + (unsigned)w * 384u + r * 8u;
#pragma unroll
        for (int q = 0; q < 3; ++q) {
          lsum[q] = xsum_g(lsum[q]);
          if (g == 0) *(AS3 f32x2*)(size_t)(sa + q * 128u) = f32x2{mrun[q], lsum[q]};
        }
      }
      bar();
      float sc[3];
      {
        const unsigned sb = ldsb + MISC + (unsigned)(w ^ 2) * 384u + r * 8u;
#pragma unroll
        for (int q = 0; q < 3; ++q) {
          const f32x2 o = *(const AS3 f32x2*)(size_t)(sb + q * 128u);
          const float mn = fmaxf(mrun[q], o[0]);
          const float a0 = __builtin_amdgcn_exp2f((mrun[q] - mn) * SM_C), a1 = __builtin_amdgcn_exp2f((o[0] - mn) * SM_C);
          const float l = lsum[q] * a0 + o[1] * a1;
          sc[q] = a0 / l;
        }
      }
      const unsigned xw = ldsb + QBUF + (unsigned)w * 9216u + lane * 16u;
      const unsigned xr = ldsb + QBUF + (unsigned)(w ^ 2) * 9216u + lane * 16u;
      f32x4 Om[6][3];
#pragma unroll
      for (int rd = 0; rd < 2; ++rd) {
#pragma unroll
        for (int d = 0; d < 3; ++d)
#pragma unroll
          for (int q = 0; q < 3; ++q) {
            const f32x4 s0 = O[3 * rd + d][q] * sc[q], s1 = O[6 + 3 * rd + d][q] * sc[q];
            stf4(xw + (unsigned)(d * 3 + q) * 1024u, kh ? s0 : s1);
            Om[3 * rd + d][q] = kh ? s1 : s0;
          }
        bar();
#pragma unroll
        for (int d = 0; d < 3; ++d)
#pragma unroll
          for (int q = 0; q < 3; ++q) Om[3 * rd + d][q] += ldf4(xr + (unsigned)(d * 3 + q) * 1024u);
        bar();
      }
      {
        char* ob = (char*)obuf + ((size_t)((b * NH + h0 + pr) * 2 + rh) * 18u) * 1024u + lane * 16u;
#pragma unroll
        for (int q = 0; q < 3; ++q)
#pragma unroll
          for (int s = 0; s < 3; ++s)
            *(bf16x8*)(ob + (size_t)(q * 6 + 3 * kh + s) * 1024u) = pack8(Om[2 * s][q], Om[2 * s + 1][q]);
      }
      STAMP(100 + pr);
    }
  } else {
    const int hw = w - 4, rh = hw & 1, dh = hw >> 1;
    const unsigned wqcol0 = (unsigned)h0 * 384u;
    const unsigned xbase = (unsigned)b * (unsigned)(SQ * DM * 2);
    const unsigned fk = (r >> 1) & 7;
    const unsigned xa0 = 128u * (48u * rh + r) + (((unsigned)g ^ fk) << 4);
    const unsigned xa1 = 128u * (48u * rh + r) + (((4u + g) ^ fk) << 4);
    const unsigned qq = r >> 2, pp = r & 3;
    const unsigned fw = ((qq >> 1) | (((unsigned)g & 1u) << 1)) << 1;
    unsigned wa[3];
#pragma unroll
    for (int s = 0; s < 3; ++s) {
      const unsigned sg = 3u * dh + s;
      wa[s] = 384u * (8u * g + qq) + ((((sg & 1u) * 4u + pp) ^ fw) << 4) + 128u * (sg >> 1);
    }
    f32x4 QA[3][2][3];
    auto zeroQA = [&]() {
#pragma unroll
      for (int s = 0; s < 3; ++s)
#pragma unroll
        for (int a = 0; a < 2; ++a)
#pragma unroll
          for (int q = 0; q < 3; ++q) QA[s][a][q] = f32x4{0.f, 0.f, 0.f, 0.f};
    };
    auto qchunk = [&](unsigned w0, unsigned w1, unsigned xs) {
      bf16x8 xf[2][3], af[4];
#pragma unroll
      for (int ks = 0; ks < 2; ++ks)
#pragma unroll
        for (int q = 0; q < 3; ++q) xf[ks][q] = ld128(ldsb + xs + (ks ? xa1 : xa0) + 2048u * q);
      auto lda = [&](int i) {
        const int ks = i / 6, s = (i % 6) >> 1, a = i & 1;
        const unsigned ad = ldsb + (ks ? w1 : w0) + wa[s] + 8u * a;
        af[i & 3] = cat(ldtr(ad), ldtr(ad + 1536u));
      };
      lda(0); lda(1); lda(2);
#pragma unroll
      for (int i = 0; i < 12; ++i) {
        SB();
        if (i + 3 < 12) lda(i + 3);
        SB();
        const int ks = i / 6, s = (i % 6) >> 1, a = i & 1;
#pragma unroll
        for (int q = 0; q < 3; ++q) QA[s][a][q] = mfma(af[i & 3], xf[ks][q], QA[s][a][q]);
      }
      SB();
    };
    auto writeQ = [&]() {
      const unsigned qw = ldsb + QBUF + (unsigned)rh * 18432u + lane * 16u;
#pragma unroll
      for (int q = 0; q < 3; ++q)
#pragma unroll
        for (int s = 0; s < 3; ++s)
          st128(qw + (unsigned)(q * 6 + 3 * dh + s) * 1024u, pack8(QA[s][0][q], QA[s][1][q]));
    };
    auto area = [&](int c) -> unsigned { const int a = (c + 2) % 3; return a == 0 ? 4u * SLOT : (a == 1 ? QBUF : 7u * SLOT); };
    auto issue_chunk = [&](unsigned pos0, unsigned pos1, unsigned pos2, int head, int c) {
      const unsigned wo = (unsigned)(64 * c) * 12288u + wqcol0 + (unsigned)head * 384u;
      issueW(pos0, wo); issueW(pos1, wo + 32u * 12288u);
      issueX(pos2, xbase + (unsigned)c * 128u);
    };
    zeroQA();
    for (int c = 0; c < 3; ++c) { const unsigned a = area(c); issue_chunk(a, a + SLOT, a + 2 * SLOT, 0, c); }
    for (int c = 0; c < 10; ++c) {
      if (c <= 7) waitvm<18>(); else if (c == 8) waitvm<9>(); else waitvm<0>();
      bar();
      STAMP(1 + c);
      if (c >= 1 && c + 2 <= 9) { const unsigned a = area(c + 2); issue_chunk(a, a + SLOT, a + 2 * SLOT, 0, c + 2); }
      const unsigned a = area(c);
      qchunk(a, a + SLOT, a + 2 * SLOT);
    }
    writeQ();
    zeroQA();
    int issued = 7;
    int curT = 1, curJ = 3;
    auto issue_upto = [&](int limit) {
      if (limit > STREAM_TOTAL) limit = STREAM_TOTAL;
      while (issued < limit) {
        const unsigned pos = (unsigned)(issued % NSLOT) * SLOT;
        const unsigned go = kvbase + (unsigned)curT * 24576u;
        if (curJ < 2) issueK(pos, go + (unsigned)curJ * SLOT);
        else if (curJ < 4) issueV(pos, go + (unsigned)(curJ - 2) * SLOT);
        else {
          const int c = curT - 1;
          if (curJ < 6) issueW(pos, (unsigned)(64 * c + 32 * (curJ - 4)) * 12288u + wqcol0 + 384u);
          else issueX(pos, xbase + (unsigned)c * 128u);
        }
        ++issued; ++curJ;
        const int len = (curT >= 1 && curT <= 10) ? 7 : 4;
        if (curJ == len) { curJ = 0; ++curT; }
      }
    };
    for (int T = 0; T < 32; ++T) {
      const int st = stream_start(T);
      const bool hasW = (T >= 1 && T <= 10);
      if (T == 16) {
        bar(); bar(); bar(); bar(); bar();
        writeQ();
      }
      if (T >= 2) wait_vm_slots(issued - 1 - (st + 1));
      STAMP(12 + 3 * T);
      bar();
      issue_upto(st + NSLOT);
      { const int last = st + (hasW ? 6 : 3); if (last >= 7) wait_vm_slots(issued - 1 - last); }
      STAMP(13 + 3 * T);
      bar();
      issue_upto(st + 2 + NSLOT);
      if (hasW) qchunk((unsigned)((st + 4) % NSLOT) * SLOT, (unsigned)((st + 5) % NSLOT) * SLOT, (unsigned)((st + 6) % NSLOT) * SLOT);
      STAMP(14 + 3 * T);
    }
    bar(); bar(); bar(); bar(); bar();
  }
  STAMP_DUMP();
}

__global__ __launch_bounds__(256) void oproj_naive(const __bf16* __restrict__ obuf, const __bf16* __restrict__ wo, __bf16* __restrict__ out) {
  const int b = blockIdx.x / 6, qblk = blockIdx.x % 6;
  __shared__ float os[16][196];
  float acc[16][3];
  for (int i = 0; i < 16; ++i) for (int j = 0; j < 3; ++j) acc[i][j] = 0.f;
  for (int h = 0; h < NH; ++h) {
    __syncthreads();
    const __bf16* src = obuf + ((size_t)((b * NH + h) * 6 + qblk) * 6) * 512;
    for (int e = threadIdx.x; e < 3072; e += 256) {
      const int ks = e >> 9, ln = (e >> 3) & 63, j = e & 7;
      const int rr = ln & 15, gg = ln >> 4;
      os[rr][32 * ks + 16 * (j >> 2) + 4 * gg + (j & 3)] = (float)src[e];
    }
    __syncthreads();
    for (int d = 0; d < DH; ++d) {
      const __bf16* wr = wo + (size_t)(h * DH + d) * DM;
      for (int j = 0; j < 3; ++j) {
        const int n = threadIdx.x + 256 * j;
        if (n < DM) { const float wv = (float)wr[n]; for (int i = 0; i < 16; ++i) acc[i][j] += os[i][d] * wv; }
      }
    }
  }
  for (int j = 0; j < 3; ++j) {
    const int n = threadIdx.x + 256 * j;
    if (n < DM) for (int i = 0; i < 16; ++i) out[((size_t)b * SQ + qblk * 16 + i) * DM + n] = (__bf16)acc[i][j];
  }
}

extern "C" void kernel_launch(void* const* d_in, const int* in_sizes, int n_in, void* d_out, int out_size, void* d_ws, size_t ws_size,
                              hipStream_t stream) {
  (void)in_sizes; (void)n_in; (void)out_size; (void)ws_size;
  static bool attr_set = false;
  if (!attr_set) { hipFuncSetAttribute((const void*)attn_kernel, hipFuncAttributeMaxDynamicSharedMemorySize, LDS_BYTES); attr_set = true; }
  attn_kernel<<<256, 512, LDS_BYTES, stream>>>(d_in[0], d_in[1], d_in[2], d_in[3], d_ws);
  oproj_naive<<<96, 256, 0, stream>>>((const __bf16*)d_ws, (const __bf16*)d_in[4], (__bf16*)d_out);
}
```

```cpp
#include <hip/hip_runtime.h>
#include <hip/hip_bf16.h>
#include <stdint.h>

typedef __bf16 bf16x8 __attribute__((ext_vector_type(8)));
typedef __bf16 bf16x4 __attribute__((ext_vector_type(4)));
typedef float f32x4 __attribute__((ext_vector_type(4)));
typedef float f32x2 __attribute__((ext_vector_type(2)));
typedef unsigned u32x4 __attribute__((ext_vector_type(4)));
typedef unsigned u32x2 __attribute__((ext_vector_type(2)));
#define AS3 __attribute__((address_space(3)))
#define AS1 __attribute__((address_space(1)))
#define DI __device__ __forceinline__
#define SB() __builtin_amdgcn_sched_barrier(0)

constexpr int NB = 16, SQ = 96, DM = 640, NH = 32, SK = 1024, DH = 192;
constexpr unsigned SLOT = 12288u, UNIT = 24576u;
constexpr unsigned BUFB = 3u * UNIT;
constexpr unsigned QBUF = 5u * UNIT;
constexpr unsigned MISC = QBUF + 36864u;
constexpr unsigned LDS_BYTES = 163840u;
constexpr unsigned KV_BYTES = (unsigned)NB * NH * SK * DH * 2u;
constexpr unsigned W_BYTES = (unsigned)DM * NH * DH * 2u;
constexpr unsigned X_BYTES = (unsigned)NB * SQ * DM * 2u;
constexpr unsigned PART_BYTES = 122880u;
constexpr unsigned WS_FLAGS = 0u;
constexpr unsigned WS_PART = 65536u;
constexpr float SM_C = 0.07216878364870322f * 1.4426950408889634f;
constexpr float RESC_THR = 6.0f / SM_C;

DI void dma16nt(unsigned ldsaddr, unsigned voff, __amdgpu_buffer_rsrc_t srd, unsigned soff) {
  asm volatile("s_mov_b32 m0, %0\n\ts_nop 0\n\tbuffer_load_dwordx4 %1, %2, %3 offen nt lds" :: "s"(ldsaddr), "v"(voff), "s"(srd), "s"(soff) : "memory");
}
template <int N> DI void waitvm() { asm volatile("s_waitcnt vmcnt(%0)" :: "i"(N) : "memory"); }
DI void bar() { asm volatile("s_waitcnt lgkmcnt(0)\n\ts_barrier" ::: "memory"); }
DI unsigned rfl(unsigned x) { return __builtin_amdgcn_readfirstlane(x); }
DI bf16x8 ld128(unsigned a) { return *(const AS3 bf16x8*)(size_t)a; }
DI f32x4 ldf4(unsigned a) { return *(const AS3 f32x4*)(size_t)a; }
DI void st128(unsigned a, bf16x8 v) { *(AS3 bf16x8*)(size_t)a = v; }
DI void stu4(unsigned a, u32x4 v) { *(AS3 u32x4*)(size_t)a = v; }
DI void stf4(unsigned a, f32x4 v) { *(AS3 f32x4*)(size_t)a = v; }
DI bf16x4 ldtr(unsigned a) { return __builtin_amdgcn_ds_read_tr16_b64_v4bf16((AS3 bf16x4*)(size_t)a); }
DI bf16x8 cat(bf16x4 lo, bf16x4 hi) { return __builtin_shufflevector(lo, hi, 0, 1, 2, 3, 4, 5, 6, 7); }
DI f32x4 mfma(bf16x8 a, bf16x8 b, f32x4 c) { return __builtin_amdgcn_mfma_f32_16x16x32_bf16(a, b, c, 0, 0, 0); }
DI u32x4 bload(__amdgpu_buffer_rsrc_t srd, unsigned voff, unsigned soff) { return __builtin_amdgcn_raw_buffer_load_b128(srd, voff, soff, 0); }
DI bf16x8 pack8(f32x4 a, f32x4 b) {
  bf16x4 lo = __builtin_convertvector(a, bf16x4), hi = __builtin_convertvector(b, bf16x4);
  return cat(lo, hi);
}
DI float xmax_g(float v) {
  auto a = __builtin_amdgcn_permlane16_swap(__float_as_uint(v), __float_as_uint(v), false, false);
  v = fmaxf(__uint_as_float(a[0]), __uint_as_float(a[1]));
  auto b = __builtin_amdgcn_permlane32_swap(__float_as_uint(v), __float_as_uint(v), false, false);
  return fmaxf(__uint_as_float(b[0]), __uint_as_float(b[1]));
}
DI float xsum_g(float v) {
  auto a = __builtin_amdgcn_permlane16_swap(__float_as_uint(v), __float_as_uint(v), false, false);
  v = __uint_as_float(a[0]) + __uint_as_float(a[1]);
  auto b = __builtin_amdgcn_permlane32_swap(__float_as_uint(v), __float_as_uint(v), false, false);
  return __uint_as_float(b[0]) + __uint_as_float(b[1]);
}
#define STAMP(i) do {} while (0)
#define STAMP_DUMP() do {} while (0)

__global__ __launch_bounds__(512, 2) void mha_kernel(const void* __restrict__ xg, const void* __restrict__ kg, const void* __restrict__ vg,
                                                     const void* __restrict__ wqg, const void* __restrict__ wog, void* __restrict__ outg,
                                                     void* __restrict__ ws) {
  extern __shared__ __attribute__((aligned(16))) char lds_raw[];
  const unsigned ldsb = (unsigned)(size_t)(AS3 char*)lds_raw;
  const int tid = threadIdx.x, lane = tid & 63;
  const int w = __builtin_amdgcn_readfirstlane(tid >> 6);
  const int r = lane & 15, g = lane >> 4;
  const int bid = blockIdx.x, xcd = bid & 7, idx = bid >> 3;
  const int b = idx >> 1, jj = 2 * xcd + (idx & 1), h0 = 2 * jj;
  const unsigned kvbase = (unsigned)(b * NH + h0) * (unsigned)(SK * DH * 2);
  const int lw = w & 3;
  STAMP(0);
  AS1 unsigned* const epw = (AS1 unsigned*)((char*)ws + WS_FLAGS) + (size_t)b * 17 * 32;
  const __amdgpu_buffer_rsrc_t srdP = __builtin_amdgcn_make_buffer_rsrc((char*)ws + WS_PART, 0, 256u * PART_BYTES, 0x00020000);

  if (w < 4) {
    const int rh = w & 1, kh = w >> 1;
    const __amdgpu_buffer_rsrc_t srdW = __builtin_amdgcn_make_buffer_rsrc((void*)wqg, 0, W_BYTES, 0x00020000);
    const __amdgpu_buffer_rsrc_t srdX = __builtin_amdgcn_make_buffer_rsrc((void*)xg, 0, X_BYTES, 0x00020000);
    const __amdgpu_buffer_rsrc_t srdO = __builtin_amdgcn_make_buffer_rsrc((void*)wog, 0, W_BYTES, 0x00020000);
    const unsigned xbase = (unsigned)b * (unsigned)(SQ * DM * 2);
    const unsigned fk = (r >> 1) & 7;
    const unsigned ka0 = ldsb + kh * SLOT + 384u * r + (((unsigned)g ^ fk) << 4);
    const unsigned ka1 = ldsb + kh * SLOT + 384u * r + (((4u + g) ^ fk) << 4);
    const unsigned qa = ldsb + QBUF + (unsigned)rh * 18432u + lane * 16u;
    const unsigned qq = r >> 2, pp = r & 3;
    const unsigned fv = (2u * (g & 1) + (qq >> 1)) << 1;
    unsigned va[4];
#pragma unroll
    for (int d = 0; d < 4; ++d) va[d] = ldsb + kh * SLOT + 384u * (4u * g + qq) + (((2u * d + (pp >> 1)) ^ fv) << 4) + 8u * (pp & 1);

#pragma unroll
    for (int pr = 0; pr < 2; ++pr) {
      {
        unsigned ln = lane; asm volatile("" : "+v"(ln));
        unsigned voW[3], voX[3];
#pragma unroll
        for (int i = 0; i < 3; ++i) {
          const unsigned p = 64u * (3u * lw + i) + ln;
          const unsigned row = p / 24u, c = p % 24u;
          voW[i] = row * 12288u + ((c ^ ((((row >> 1) & 1u) | (((row >> 3) & 1u) << 1)) << 1)) << 4);
          const unsigned rx = p >> 3, cx = p & 7u;
          voX[i] = rx * 1280u + ((cx ^ ((rx >> 1) & 7u)) << 4);
        }
        const unsigned stw = ldsb + 3072u * lw + ln * 16u;
        const unsigned wq0 = (unsigned)(h0 + pr) * 384u;
        u32x4 R[2][9];
        auto ldc = [&](int s, int c) {
          const unsigned wo = (unsigned)(64 * c) * 12288u + wq0;
#pragma unroll
          for (int i = 0; i < 3; ++i) R[s][i] = bload(srdW, voW[i], wo);
#pragma unroll
          for (int i = 0; i < 3; ++i) R[s][3 + i] = bload(srdW, voW[i], wo + 32u * 12288u);
#pragma unroll
          for (int i = 0; i < 3; ++i) R[s][6 + i] = bload(srdX, voX[i], xbase + (unsigned)c * 128u);
        };
        auto stc = [&](int s, int c) {
          const unsigned base = stw + ((c & 1) ? BUFB : QBUF);
#pragma unroll
          for (int j = 0; j < 3; ++j)
#pragma unroll
            for (int i = 0; i < 3; ++i) stu4(base + (unsigned)j * SLOT + 1024u * i, R[s][3 * j + i]);
        };
        ldc(0, 0); ldc(1, 1);
        stc(0, 0); ldc(0, 2);
        bar();
#pragma unroll
        for (int c = 0; c < 9; ++c) {
          stc((c + 1) & 1, c + 1);
          if (c + 3 <= 9) ldc((c + 1) & 1, c + 3);
          bar();
        }
      }
      STAMP(1 + pr * 110);
      f32x4 O[12][3];
#pragma unroll
      for (int d = 0; d < 12; ++d)
#pragma unroll
        for (int q = 0; q < 3; ++q) O[d][q] = f32x4{0.f, 0.f, 0.f, 0.f};
      float mrun[3] = {-INFINITY, -INFINITY, -INFINITY}, lsum[3] = {0.f, 0.f, 0.f};
      for (int t = 0; t < 16; ++t) {
        const unsigned kpos = (unsigned)((2 * t) & 3) * UNIT, vpos = (unsigned)((2 * t + 1) & 3) * UNIT;
        bar();
        STAMP(2 + 3 * (pr * 16 + t));
        f32x4 S[2][3];
#pragma unroll
        for (int q = 0; q < 3; ++q) { S[0][q] = f32x4{0.f, 0.f, 0.f, 0.f}; S[1][q] = f32x4{0.f, 0.f, 0.f, 0.f}; }
        const unsigned k0 = ka0 + kpos, k1 = ka1 + kpos;
        {
          bf16x8 kf[3][2], qf[3][3];
          auto ldstep = [&](int s) {
            const unsigned kb = ((s & 1) ? k1 : k0) + 128u * (s >> 1);
            kf[s % 3][0] = ld128(kb); kf[s % 3][1] = ld128(kb + 6144u);
#pragma unroll
            for (int q = 0; q < 3; ++q) qf[s % 3][q] = ld128(qa + (unsigned)(q * 6 + s) * 1024u);
          };
          ldstep(0); ldstep(1);
#pragma unroll
          for (int s = 0; s < 6; ++s) {
            SB();
            if (s + 2 < 6) ldstep(s + 2);
            SB();
#pragma unroll
            for (int q = 0; q < 3; ++q) {
              S[0][q] = mfma(kf[s % 3][0], qf[s % 3][q], S[0][q]);
              S[1][q] = mfma(kf[s % 3][1], qf[s % 3][q], S[1][q]);
            }
          }
          SB();
        }
        float mx[3];
        bool need = false;
#pragma unroll
        for (int q = 0; q < 3; ++q) {
          float m = fmaxf(fmaxf(fmaxf(S[0][q][0], S[0][q][1]), fmaxf(S[0][q][2], S[0][q][3])),
                          fmaxf(fmaxf(S[1][q][0], S[1][q][1]), fmaxf(S[1][q][2], S[1][q][3])));
          mx[q] = xmax_g(m);
          need |= (mx[q] > mrun[q] + RESC_THR);
        }
        if (__any(need)) {
#pragma unroll
          for (int q = 0; q < 3; ++q) {
            const float mn = fmaxf(mrun[q], mx[q]);
            const float al = __builtin_amdgcn_exp2f((mrun[q] - mn) * SM_C);
            mrun[q] = mn;
            lsum[q] *= al;
#pragma unroll
            for (int d = 0; d < 12; ++d) O[d][q] *= al;
          }
        }
        bf16x8 pf[3];
#pragma unroll
        for (int q = 0; q < 3; ++q) {
          const float nm = -mrun[q] * SM_C;
          f32x4 p0, p1;
#pragma unroll
          for (int i = 0; i < 4; ++i) {
            p0[i] = __builtin_amdgcn_exp2f(fmaf(S[0][q][i], SM_C, nm));
            p1[i] = __builtin_amdgcn_exp2f(fmaf(S[1][q][i], SM_C, nm));
          }
          lsum[q] += ((p0[0] + p0[1]) + (p0[2] + p0[3])) + ((p1[0] + p1[1]) + (p1[2] + p1[3]));
          pf[q] = pack8(p0, p1);
        }
        STAMP(3 + 3 * (pr * 16 + t));
        bar();
        STAMP(4 + 3 * (pr * 16 + t));
        {
          bf16x8 vf[4];
          auto ldv = [&](int d) {
            const unsigned a = va[d & 3] + vpos + 128u * (d >> 2);
            vf[d & 3] = cat(ldtr(a), ldtr(a + 6144u));
          };
          ldv(0); ldv(1); ldv(2);
#pragma unroll
          for (int d = 0; d < 12; ++d) {
            SB();
            if (d + 3 < 12) ldv(d + 3);
            SB();
#pragma unroll
            for (int q = 0; q < 3; ++q) O[d][q] = mfma(vf[d & 3], pf[q], O[d][q]);
          }
          SB();
        }
      }
      {
        const unsigned sa = ldsb + MISC + (unsigned)w * 384u + r * 8u;
#pragma unroll
        for (int q = 0; q < 3; ++q) {
          lsum[q] = xsum_g(lsum[q]);
          if (g == 0) *(AS3 f32x2*)(size_t)(sa + q * 128u) = f32x2{mrun[q], lsum[q]};
        }
      }
      bar();
      float sc[3];
      {
        const unsigned sb = ldsb + MISC + (unsigned)(w ^ 2) * 384u + r * 8u;
#pragma unroll
        for (int q = 0; q < 3; ++q) {
          const f32x2 o = *(const AS3 f32x2*)(size_t)(sb + q * 128u);
          const float mn = fmaxf(mrun[q], o[0]);
          const float a0 = __builtin_amdgcn_exp2f((mrun[q] - mn) * SM_C), a1 = __builtin_amdgcn_exp2f((o[0] - mn) * SM_C);
          const float l = lsum[q] * a0 + o[1] * a1;
          sc[q] = a0 / l;
        }
      }
      const unsigned xw = ldsb + BUFB + (unsigned)w * 18432u + lane * 16u;
      const unsigned xr = ldsb + BUFB + (unsigned)(w ^ 2) * 18432u + lane * 16u;
      if (kh) {
#pragma unroll
        for (int d = 0; d < 6; ++d)
#pragma unroll
          for (int q = 0; q < 3; ++q) { stf4(xw + (unsigned)(d * 3 + q) * 1024u, O[d][q] * sc[q]); O[d][q] = O[6 + d][q] * sc[q]; }
      } else {
#pragma unroll
        for (int d = 0; d < 6; ++d)
#pragma unroll
          for (int q = 0; q < 3; ++q) { stf4(xw + (unsigned)(d * 3 + q) * 1024u, O[6 + d][q] * sc[q]); O[d][q] = O[d][q] * sc[q]; }
      }
      bar();
#pragma unroll
      for (int d = 0; d < 6; ++d)
#pragma unroll
        for (int q = 0; q < 3; ++q) O[d][q] += ldf4(xr + (unsigned)(d * 3 + q) * 1024u);
      asm volatile("s_waitcnt lgkmcnt(0)" ::: "memory");
#pragma unroll
      for (int q = 0; q < 3; ++q)
#pragma unroll
        for (int s = 0; s < 3; ++s) st128(xr + (unsigned)(q * 3 + s) * 1024u, pack8(O[2 * s][q], O[2 * s + 1][q]));
      bar();
      STAMP(100 + pr);
      bar();
    }
    {
      unsigned ln = lane; asm volatile("" : "+v"(ln));
      const unsigned fo = ((ln >> 4) & 3u) << 1;
      const unsigned voO = (ln >> 3) * 1280u + (((ln & 7u) ^ fo) << 4);
      const unsigned wobase = (unsigned)h0 * 192u * 1280u + (unsigned)lw * 12u * 8u * 1280u;
      const unsigned sto = ldsb + (unsigned)lw * 12288u + ln * 16u;
      u32x4 R[2][12];
      auto ldc = [&](int s, int c) {
#pragma unroll
        for (int i = 0; i < 12; ++i) R[s][i] = bload(srdO, voO, wobase + (unsigned)i * 10240u + (unsigned)c * 128u);
      };
      auto stc = [&](int s, int c) {
#pragma unroll
        for (int i = 0; i < 12; ++i) stu4(sto + (unsigned)(c & 1) * 49152u + 1024u * i, R[s][i]);
      };
      ldc(0, 0); ldc(1, 1);
      stc(0, 0); ldc(0, 2);
      bar();
#pragma unroll
      for (int c = 0; c < 9; ++c) {
        stc((c + 1) & 1, c + 1);
        if (c + 3 <= 9) ldc((c + 1) & 1, c + 3);
        bar();
      }
    }
  } else {
    const int hw = w - 4, rh = hw & 1, xh = hw >> 1;
    const __amdgpu_buffer_rsrc_t srdK = __builtin_amdgcn_make_buffer_rsrc((void*)kg, 0, KV_BYTES, 0x00020000);
    const __amdgpu_buffer_rsrc_t srdV = __builtin_amdgcn_make_buffer_rsrc((void*)vg, 0, KV_BYTES, 0x00020000);
    unsigned voK[6], voV[6];
#pragma unroll
    for (int i = 0; i < 6; ++i) {
      const unsigned p = 64u * i + lane;
      const unsigned row = p / 24u, c = p % 24u;
      voK[i] = (row + 16u * hw) * 384u + ((c ^ ((row >> 1) & 7u)) << 4);
      voV[i] = (row + 16u * hw) * 384u + ((c ^ (((row >> 1) & 3u) << 1)) << 4);
    }
    const unsigned ldsw = ldsb + 6144u * hw;
    auto issue = [&](int u) {
      const unsigned pos = ldsw + (unsigned)(u & 3) * UNIT;
      const unsigned go = kvbase + (unsigned)(u >> 1) * UNIT;
      if (u & 1) {
#pragma unroll
        for (int i = 0; i < 6; ++i) dma16nt(rfl(pos + 1024u * i), voV[i], srdV, rfl(go));
      } else {
#pragma unroll
        for (int i = 0; i < 6; ++i) dma16nt(rfl(pos + 1024u * i), voK[i], srdK, rfl(go));
      }
    };
    const unsigned fk = (r >> 1) & 7;
    const unsigned xa0 = 128u * (48u * rh + r) + (((unsigned)g ^ fk) << 4);
    const unsigned xa1 = 128u * (48u * rh + r) + (((4u + g) ^ fk) << 4);
    const unsigned qq = r >> 2, pp = r & 3;
    const unsigned fw = ((qq >> 1) | (((unsigned)g & 1u) << 1)) << 1;
    unsigned wa[3];
#pragma unroll
    for (int s = 0; s < 3; ++s) {
      const unsigned sg = 3u * xh + s;
      wa[s] = 384u * (8u * g + qq) + ((((sg & 1u) * 4u + pp) ^ fw) << 4) + 128u * (sg >> 1);
    }
    bf16x8 Of[2][6][3];
    const unsigned E = __hip_atomic_load(epw, __ATOMIC_RELAXED, __HIP_MEMORY_SCOPE_AGENT);

#pragma unroll
    for (int pr = 0; pr < 2; ++pr) {
      f32x4 QA[3][2][3];
#pragma unroll
      for (int s = 0; s < 3; ++s)
#pragma unroll
        for (int a = 0; a < 2; ++a)
#pragma unroll
          for (int q = 0; q < 3; ++q) QA[s][a][q] = f32x4{0.f, 0.f, 0.f, 0.f};
      for (int c = 0; c < 10; ++c) {
        bar();
        STAMP(102 + c);
        if (pr == 0) { if (c == 0) issue(0); else if (c == 3) issue(1); else if (c == 6) issue(2); }
        const unsigned base = ldsb + ((c & 1) ? BUFB : QBUF);
        bf16x8 xf[2][3], af[4];
#pragma unroll
        for (int ks = 0; ks < 2; ++ks)
#pragma unroll
          for (int q = 0; q < 3; ++q) xf[ks][q] = ld128(base + 2u * SLOT + (ks ? xa1 : xa0) + 2048u * q);
        auto lda = [&](int i) {
          const int ks = i / 6, s = (i % 6) >> 1, a = i & 1;
          const unsigned ad = base + (unsigned)ks * SLOT + wa[s] + 8u * a;
          af[i & 3] = cat(ldtr(ad), ldtr(ad + 1536u));
        };
        lda(0); lda(1); lda(2);
#pragma unroll
        for (int i = 0; i < 12; ++i) {
          SB();
          if (i + 3 < 12) lda(i + 3);
          SB();
          const int ks = i / 6, s = (i % 6) >> 1, a = i & 1;
#pragma unroll
          for (int q = 0; q < 3; ++q) QA[s][a][q] = mfma(af[i & 3], xf[ks][q], QA[s][a][q]);
        }
        SB();
      }
      {
        const unsigned qw = ldsb + QBUF + (unsigned)rh * 18432u + lane * 16u;
#pragma unroll
        for (int q = 0; q < 3; ++q)
#pragma unroll
          for (int s = 0; s < 3; ++s)
            st128(qw + (unsigned)(q * 6 + 3 * xh + s) * 1024u, pack8(QA[s][0][q], QA[s][1][q]));
      }
      for (int t = 0; t < 16; ++t) {
        const int T = pr * 16 + t;
        if (T < 31) waitvm<12>(); else waitvm<6>();
        STAMP(12 + 2 * T);
        bar();
        if (2 * T + 3 < 64) issue(2 * T + 3);
        if (T < 31) waitvm<12>(); else waitvm<0>();
        STAMP(13 + 2 * T);
        bar();
        if (2 * T + 4 < 64) issue(2 * T + 4);
      }
      bar(); bar(); bar();
      {
#pragma unroll
        for (int k2 = 0; k2 < 2; ++k2) {
          const unsigned src = ldsb + BUFB + (unsigned)(rh + 2 * (1 - k2)) * 18432u + lane * 16u;
#pragma unroll
          for (int q = 0; q < 3; ++q)
#pragma unroll
            for (int s = 0; s < 3; ++s) {
              const bf16x8 f = ld128(src + (unsigned)(q * 3 + s) * 1024u);
              if (pr == 0) Of[0][3 * k2 + s][q] = f; else Of[1][3 * k2 + s][q] = f;
            }
        }
      }
      bar();
    }
    {
      const unsigned fv = (2u * (g & 1) + (qq >> 1)) << 1;
      unsigned oa[2];
#pragma unroll
      for (int nb = 0; nb < 2; ++nb) oa[nb] = ldsb + 128u * (4u * g + qq) + (((4u * xh + 2u * nb + (pp >> 1)) ^ fv) << 4) + 8u * (pp & 1);
      const unsigned pbase = (unsigned)bid * PART_BYTES + (unsigned)hw * 30720u;
      for (int c = 0; c < 10; ++c) {
        bar();
        STAMP(114 + c);
        f32x4 acc[2][3];
#pragma unroll
        for (int nb = 0; nb < 2; ++nb)
#pragma unroll
          for (int q = 0; q < 3; ++q) acc[nb][q] = f32x4{0.f, 0.f, 0.f, 0.f};
        const unsigned cb = (unsigned)(c & 1) * 49152u;
        bf16x8 af[4];
        auto lda = [&](int i) {
          const unsigned ad = oa[i & 1] + cb + 4096u * (i >> 1);
          af[i & 3] = cat(ldtr(ad), ldtr(ad + 2048u));
        };
        lda(0); lda(1); lda(2);
#pragma unroll
        for (int i = 0; i < 24; ++i) {
          SB();
          if (i + 3 < 24) lda(i + 3);
          SB();
          const int ks = i >> 1;
#pragma unroll
          for (int q = 0; q < 3; ++q) acc[i & 1][q] = mfma(af[i & 3], Of[ks / 6][ks % 6][q], acc[i & 1][q]);
        }
        SB();
#pragma unroll
        for (int q = 0; q < 3; ++q)
          __builtin_amdgcn_raw_buffer_store_b128(__builtin_bit_cast(u32x4, pack8(acc[0][q], acc[1][q])), srdP, lane * 16u,
                                                 pbase + (unsigned)(c * 3 + q) * 1024u, 16  );
      }
      waitvm<0>();
    }
    bar();
    const unsigned tag = E + 1u;
    if (hw == 0) {
      if (lane == 0) __hip_atomic_store(epw + 32 * (1 + jj), tag, __ATOMIC_RELAXED, __HIP_MEMORY_SCOPE_AGENT);
      for (unsigned spin = 0; spin < (1u << 22); ++spin) {
        const unsigned v = __hip_atomic_load(epw + 32 * (1 + (lane & 15)), __ATOMIC_RELAXED, __HIP_MEMORY_SCOPE_AGENT);
        if (__all(v == tag)) break;
        __builtin_amdgcn_s_sleep(1);
      }
      if (jj == 0 && lane == 0) __hip_atomic_store(epw, tag, __ATOMIC_RELAXED, __HIP_MEMORY_SCOPE_AGENT);
      __builtin_amdgcn_fence(__ATOMIC_ACQUIRE, "agent");
      waitvm<0>();
    }
  }
  if (w < 4) bar();
  bar();
  STAMP(126);
  {
    const int piece = jj * 8 + w;
    if (piece < 120) {
      const unsigned src = (unsigned)(b * 32) * PART_BYTES;
      (void)src;
      float s[8];
#pragma unroll
      for (int i = 0; i < 8; ++i) s[i] = 0.f;
      u32x4 v[16];
#pragma unroll
      for (int k = 0; k < 16; ++k) {
        const unsigned obid = (unsigned)(((b * 2 + (k & 1)) << 3) | (k >> 1));
        v[k] = bload(srdP, lane * 16u, obid * PART_BYTES + (unsigned)piece * 1024u);
      }
#pragma unroll
      for (int k = 0; k < 16; ++k)
#pragma unroll
        for (int i = 0; i < 4; ++i) { s[2 * i] += __uint_as_float(v[k][i] << 16); s[2 * i + 1] += __uint_as_float(v[k][i] & 0xffff0000u); }
      const int hp = piece / 30, rem = piece % 30, c = rem / 3, q = rem % 3;
      const int row = 48 * (hp & 1) + 16 * q + r, n0 = 64 * c + 32 * (hp >> 1) + 4 * g;
      __bf16* op = (__bf16*)outg + ((size_t)b * SQ + row) * DM + n0;
      *(bf16x4*)op = __builtin_convertvector((f32x4{s[0], s[1], s[2], s[3]}), bf16x4);
      *(bf16x4*)(op + 16) = __builtin_convertvector((f32x4{s[4], s[5], s[6], s[7]}), bf16x4);
    }
  }
  STAMP(127);
  STAMP_DUMP();
}

extern "C" void kernel_launch(void* const* d_in, const int* in_sizes, int n_in, void* d_out, int out_size, void* d_ws, size_t ws_size,
                              hipStream_t stream) {
  (void)in_sizes; (void)n_in; (void)out_size; (void)ws_size;
  (void)hipFuncSetAttribute((const void*)mha_kernel, hipFuncAttributeMaxDynamicSharedMemorySize, LDS_BYTES);
  mha_kernel<<<256, 512, LDS_BYTES, stream>>>(d_in[0], d_in[1], d_in[2], d_in[3], d_in[4], d_out, d_ws);
}
```

```cpp
#include <hip/hip_runtime.h>
#include <hip/hip_bf16.h>
#include <stdint.h>
#define WNT 1

typedef __bf16 bf16x8 __attribute__((ext_vector_type(8)));
typedef __bf16 bf16x4 __attribute__((ext_vector_type(4)));
typedef float f32x4 __attribute__((ext_vector_type(4)));
typedef float f32x2 __attribute__((ext_vector_type(2)));
typedef unsigned u32x4 __attribute__((ext_vector_type(4)));
typedef unsigned u32x2 __attribute__((ext_vector_type(2)));
#define AS3 __attribute__((address_space(3)))
#define AS1 __attribute__((address_space(1)))
#define DI __device__ __forceinline__
#define SB() __builtin_amdgcn_sched_barrier(0)

constexpr int NB = 16, SQ = 96, DM = 640, NH = 32, SK = 1024, DH = 192;
constexpr unsigned SLOT = 12288u, UNIT = 24576u;
constexpr unsigned WAREA = 3u * UNIT;
constexpr unsigned QBUF = 5u * UNIT;
constexpr unsigned MISC = QBUF + 36864u;
constexpr unsigned LDS_BYTES = 163840u;
constexpr unsigned KV_BYTES = (unsigned)NB * NH * SK * DH * 2u;
constexpr unsigned W_BYTES = (unsigned)DM * NH * DH * 2u;
constexpr unsigned X_BYTES = (unsigned)NB * SQ * DM * 2u;
constexpr unsigned PART_BYTES = 122880u;
constexpr unsigned WS_FLAGS = 0u;
constexpr unsigned WS_PART = 65536u;
constexpr float SM_C = 0.07216878364870322f * 1.4426950408889634f;
constexpr float RESC_THR = 6.0f / SM_C;

DI void dma16(unsigned ldsaddr, unsigned voff, __amdgpu_buffer_rsrc_t srd, unsigned soff) {
  asm volatile("s_mov_b32 m0, %0\n\ts_nop 0\n\tbuffer_load_dwordx4 %1, %2, %3 offen nt lds" :: "s"(ldsaddr), "v"(voff), "s"(srd), "s"(soff) : "memory");
}
DI void dma16nt(unsigned ldsaddr, unsigned voff, __amdgpu_buffer_rsrc_t srd, unsigned soff) {
  asm volatile("s_mov_b32 m0, %0\n\ts_nop 0\n\tbuffer_load_dwordx4 %1, %2, %3 offen nt lds" :: "s"(ldsaddr), "v"(voff), "s"(srd), "s"(soff) : "memory");
}
template <int N> DI void waitvm() { asm volatile("s_waitcnt vmcnt(%0)" :: "i"(N) : "memory"); }
DI void wait_vm_dyn(int n) {
  if (n >= 12) waitvm<12>(); else if (n == 11) waitvm<11>(); else if (n >= 6) waitvm<6>(); else if (n == 5) waitvm<5>(); else waitvm<0>();
}
DI void bar() { asm volatile("s_waitcnt lgkmcnt(0)\n\ts_barrier" ::: "memory"); }
DI unsigned lane_id() { unsigned l; asm volatile("v_mbcnt_lo_u32_b32 %0, -1, 0\n\tv_mbcnt_hi_u32_b32 %0, -1, %0" : "=v"(l)); return l; }
DI unsigned rfl(unsigned x) { return __builtin_amdgcn_readfirstlane(x); }
DI bf16x8 ld128(unsigned a) { return *(const AS3 bf16x8*)(size_t)a; }
DI f32x4 ldf4(unsigned a) { return *(const AS3 f32x4*)(size_t)a; }
DI void st128(unsigned a, bf16x8 v) { *(AS3 bf16x8*)(size_t)a = v; }
DI void stu4(unsigned a, u32x4 v) { *(AS3 u32x4*)(size_t)a = v; }
DI void stf4(unsigned a, f32x4 v) { *(AS3 f32x4*)(size_t)a = v; }
DI bf16x4 ldtr(unsigned a) { return __builtin_amdgcn_ds_read_tr16_b64_v4bf16((AS3 bf16x4*)(size_t)a); }
DI bf16x8 cat(bf16x4 lo, bf16x4 hi) { return __builtin_shufflevector(lo, hi, 0, 1, 2, 3, 4, 5, 6, 7); }
DI f32x4 mfma(bf16x8 a, bf16x8 b, f32x4 c) { return __builtin_amdgcn_mfma_f32_16x16x32_bf16(a, b, c, 0, 0, 0); }
DI u32x4 bload(__amdgpu_buffer_rsrc_t srd, unsigned voff, unsigned soff) {
  return __builtin_amdgcn_raw_buffer_load_b128(srd, voff, soff, 2);
}
DI bf16x8 pack8(f32x4 a, f32x4 b) {
  bf16x4 lo = __builtin_convertvector(a, bf16x4), hi = __builtin_convertvector(b, bf16x4);
  return cat(lo, hi);
}
DI f32x4 unpack4(bf16x4 v) {
  const u32x2 u = __builtin_bit_cast(u32x2, v);
  return f32x4{__uint_as_float(u[0] << 16), __uint_as_float(u[0] & 0xffff0000u), __uint_as_float(u[1] << 16), __uint_as_float(u[1] & 0xffff0000u)};
}
DI float max3f(float a, float b, float c) { float r; asm("v_max3_f32 %0, %1, %2, %3" : "=v"(r) : "v"(a), "v"(b), "v"(c)); return r; }
DI float max2f(float a, float b) { float r; asm("v_max_f32_e32 %0, %1, %2" : "=v"(r) : "v"(a), "v"(b)); return r; }
DI float xmax_g(float v) {
  auto a = __builtin_amdgcn_permlane16_swap(__float_as_uint(v), __float_as_uint(v), false, false);
  v = fmaxf(__uint_as_float(a[0]), __uint_as_float(a[1]));
  auto b = __builtin_amdgcn_permlane32_swap(__float_as_uint(v), __float_as_uint(v), false, false);
  return fmaxf(__uint_as_float(b[0]), __uint_as_float(b[1]));
}
DI float xsum_g(float v) {
  auto a = __builtin_amdgcn_permlane16_swap(__float_as_uint(v), __float_as_uint(v), false, false);
  v = __uint_as_float(a[0]) + __uint_as_float(a[1]);
  auto b = __builtin_amdgcn_permlane32_swap(__float_as_uint(v), __float_as_uint(v), false, false);
  return __uint_as_float(b[0]) + __uint_as_float(b[1]);
}
#define STAMP(i) do {} while (0)
#define STAMP_DUMP() do {} while (0)

__global__ __launch_bounds__(512, 2) void mha_kernel(const void* __restrict__ xg, const void* __restrict__ kg, const void* __restrict__ vg,
                                                     const void* __restrict__ wqg, const void* __restrict__ wog, void* __restrict__ outg,
                                                     void* __restrict__ ws) {
  extern __shared__ __attribute__((aligned(16))) char lds_raw[];
  const unsigned ldsb = (unsigned)(size_t)(AS3 char*)lds_raw;
  const int tid = threadIdx.x, lane = tid & 63;
  const int w = __builtin_amdgcn_readfirstlane(tid >> 6);
  const int r = lane & 15, g = lane >> 4;
  const int bid = blockIdx.x, xcd = bid & 7, idx = bid >> 3;
  const int b = idx >> 1, jj = 2 * xcd + (idx & 1), h0 = 2 * jj;
  const unsigned kvbase = (unsigned)(b * NH + h0) * (unsigned)(SK * DH * 2);
  const int lw = w & 3;
  STAMP(0);
  AS1 unsigned* const epw = (AS1 unsigned*)((char*)ws + WS_FLAGS) + (size_t)b * 17 * 32;
  const __amdgpu_buffer_rsrc_t srdP = __builtin_amdgcn_make_buffer_rsrc((char*)ws + WS_PART, 0, 256u * PART_BYTES, 0x00020000);
  const __amdgpu_buffer_rsrc_t srdW = __builtin_amdgcn_make_buffer_rsrc((void*)wqg, 0, W_BYTES, 0x00020000);
  const __amdgpu_buffer_rsrc_t srdX = __builtin_amdgcn_make_buffer_rsrc((void*)xg, 0, X_BYTES, 0x00020000);
  const __amdgpu_buffer_rsrc_t srdO = __builtin_amdgcn_make_buffer_rsrc((void*)wog, 0, W_BYTES, 0x00020000);
  const unsigned xbase = (unsigned)b * (unsigned)(SQ * DM * 2);
  auto obuf_addr = [&](int c) -> unsigned { const int k = c % 6; return k < 2 ? WAREA + (unsigned)k * UNIT : (k < 5 ? (unsigned)(k - 2) * UNIT : QBUF); };

  if (w < 4) {
    const int rh = w & 1, kh = w >> 1;
    {
      unsigned voW[3], voX[3];
#pragma unroll
      for (int i = 0; i < 3; ++i) {
        const unsigned p = 64u * (3u * lw + i) + lane;
        const unsigned row = p / 24u, c = p % 24u;
        voW[i] = row * 12288u + ((c ^ ((((row >> 1) & 1u) | (((row >> 3) & 1u) << 1)) << 1)) << 4);
        const unsigned rx = p >> 3, cx = p & 7u;
        voX[i] = rx * 1280u + ((cx ^ ((rx >> 1) & 7u)) << 4);
      }
      const unsigned stw = ldsb + 3072u * lw + lane * 16u;
      const unsigned wq0 = (unsigned)h0 * 384u;
      constexpr int QD = 5;
      u32x4 R[QD][9];
      auto ldc = [&](int s, int c) {
        const unsigned wo = (unsigned)(64 * c) * 12288u + wq0;
#pragma unroll
        for (int i = 0; i < 3; ++i) R[s][i] = bload(srdW, voW[i], wo);
#pragma unroll
        for (int i = 0; i < 3; ++i) R[s][3 + i] = bload(srdW, voW[i], wo + 32u * 12288u);
#pragma unroll
        for (int i = 0; i < 3; ++i) R[s][6 + i] = bload(srdX, voX[i], xbase + (unsigned)c * 128u);
      };
      auto stc = [&](int s, int c) {
        const unsigned base = stw + ((c & 1) ? WAREA : QBUF);
#pragma unroll
        for (int j = 0; j < 3; ++j)
#pragma unroll
          for (int i = 0; i < 3; ++i) stu4(base + (unsigned)j * SLOT + 1024u * i, R[s][3 * j + i]);
      };
#pragma unroll
      for (int c = 0; c < QD; ++c) ldc(c, c);
      SB();
      stc(0, 0); ldc(0, QD);
      bar();
#pragma unroll
      for (int c = 0; c < 9; ++c) {
        stc((c + 1) % QD, c + 1);
        if (c + 1 + QD <= 9) ldc((c + 1) % QD, c + 1 + QD);
        bar();
      }
    }
    STAMP(1);
    const unsigned ln = lane_id();
    const unsigned r2 = ln & 15u, g2 = ln >> 4;
    const unsigned fk = (r2 >> 1) & 7;
    const unsigned ka0 = ldsb + kh * SLOT + 384u * r2 + ((g2 ^ fk) << 4);
    const unsigned ka1 = ldsb + kh * SLOT + 384u * r2 + (((4u + g2) ^ fk) << 4);
    const unsigned qa = ldsb + QBUF + (unsigned)rh * 18432u + ln * 16u;
    const unsigned qq = r2 >> 2, pp = r2 & 3;
    const unsigned fv = (2u * (g2 & 1) + (qq >> 1)) << 1;
    unsigned va[4];
#pragma unroll
    for (int d = 0; d < 4; ++d) va[d] = ldsb + kh * SLOT + 384u * (4u * g2 + qq) + (((2u * d + (pp >> 1)) ^ fv) << 4) + 8u * (pp & 1);

#pragma unroll
    for (int pr = 0; pr < 2; ++pr) {
      f32x4 O[12][3];
#pragma unroll
      for (int d = 0; d < 12; ++d)
#pragma unroll
        for (int q = 0; q < 3; ++q) O[d][q] = f32x4{0.f, 0.f, 0.f, 0.f};
      float mrun[3] = {-INFINITY, -INFINITY, -INFINITY};
      f32x2 lsum2[3] = {f32x2{0.f, 0.f}, f32x2{0.f, 0.f}, f32x2{0.f, 0.f}};
      unsigned kpos = (unsigned)((32 * pr) % 3) * UNIT;
      for (int t = 0; t < 16; ++t) {
        const unsigned vpos = kpos == 2u * UNIT ? 0u : kpos + UNIT;
        f32x4 S[2][3];
        {
          bf16x8 kf[3][2], qf[3][3];
          auto ldq = [&](int s) {
#pragma unroll
            for (int q = 0; q < 3; ++q) qf[s % 3][q] = ld128(qa + (unsigned)(q * 6 + s) * 1024u);
          };
          const unsigned k0 = ka0 + kpos, k1 = ka1 + kpos;
          auto ldk = [&](int s) {
            const unsigned kb = ((s & 1) ? k1 : k0) + 128u * (s >> 1);
            kf[s % 3][0] = ld128(kb); kf[s % 3][1] = ld128(kb + 6144u);
          };
          if (t > 0) { ldq(0); ldq(1); }
          asm volatile("s_barrier" ::: "memory");
          STAMP(2 + 3 * (pr * 16 + t));
          if (t == 0) { ldq(0); ldq(1); }
          ldk(0); ldk(1);
#pragma unroll
          for (int q = 0; q < 3; ++q) { S[0][q] = f32x4{0.f, 0.f, 0.f, 0.f}; S[1][q] = f32x4{0.f, 0.f, 0.f, 0.f}; }
#pragma unroll
          for (int s = 0; s < 6; ++s) {
            SB();
            if (s + 2 < 6) { ldk(s + 2); ldq(s + 2); }
            SB();
#pragma unroll
            for (int q = 0; q < 3; ++q) {
              S[0][q] = mfma(kf[s % 3][0], qf[s % 3][q], S[0][q]);
              S[1][q] = mfma(kf[s % 3][1], qf[s % 3][q], S[1][q]);
            }
          }
          SB();
        }
        bar();
        STAMP(4 + 3 * (pr * 16 + t));
        bf16x8 vf[7];
        auto ldv = [&](int d) {
          const unsigned a = va[d & 3] + vpos + 128u * (d >> 2);
          vf[d % 7] = cat(ldtr(a), ldtr(a + 6144u));
        };
        ldv(0); ldv(1); ldv(2); ldv(3); ldv(4); ldv(5);
        SB();
        float mx[3];
        bool need = false;
#pragma unroll
        for (int q = 0; q < 3; ++q) {
          mx[q] = max3f(max3f(S[0][q][0], S[0][q][1], S[0][q][2]), max3f(S[0][q][3], S[1][q][0], S[1][q][1]), max2f(S[1][q][2], S[1][q][3]));
          need |= (mx[q] > mrun[q] + RESC_THR);
        }
        if (__any(need)) {
#pragma unroll
          for (int q = 0; q < 3; ++q) {
            const float mn = fmaxf(mrun[q], xmax_g(mx[q]));
            const float al = __builtin_amdgcn_exp2f((mrun[q] - mn) * SM_C);
            mrun[q] = mn;
            lsum2[q] *= al;
#pragma unroll
            for (int d = 0; d < 12; ++d) O[d][q] *= al;
          }
        }
        bf16x8 pf[3];
#pragma unroll
        for (int q = 0; q < 3; ++q) {
          const float nm = -mrun[q] * SM_C;
          const f32x2 nm2 = {nm, nm}, c2 = {SM_C, SM_C};
          f32x2 e[4];
#pragma unroll
          for (int i = 0; i < 4; ++i) {
            const f32x2 sv = {S[i >> 1][q][2 * (i & 1)], S[i >> 1][q][2 * (i & 1) + 1]};
            const f32x2 a = sv * c2 + nm2;
            e[i] = f32x2{__builtin_amdgcn_exp2f(a[0]), __builtin_amdgcn_exp2f(a[1])};
          }
          lsum2[q] += (e[0] + e[1]) + (e[2] + e[3]);
          pf[q] = pack8(f32x4{e[0][0], e[0][1], e[1][0], e[1][1]}, f32x4{e[2][0], e[2][1], e[3][0], e[3][1]});
        }
        STAMP(3 + 3 * (pr * 16 + t));
#pragma unroll
        for (int d = 0; d < 12; ++d) {
          SB();
          if (d + 6 < 12) ldv(d + 6);
          SB();
#pragma unroll
          for (int q = 0; q < 3; ++q) O[d][q] = mfma(vf[d % 7], pf[q], O[d][q]);
        }
        SB();
        asm volatile("s_waitcnt lgkmcnt(0)" ::: "memory");
        kpos = vpos == 2u * UNIT ? 0u : vpos + UNIT;
      }
      float lsum[3];
      {
        const unsigned sa = ldsb + MISC + (unsigned)w * 384u + r2 * 8u;
#pragma unroll
        for (int q = 0; q < 3; ++q) {
          lsum[q] = xsum_g(lsum2[q][0] + lsum2[q][1]);
          if (g2 == 0) *(AS3 f32x2*)(size_t)(sa + q * 128u) = f32x2{mrun[q], lsum[q]};
        }
      }
      bar();
      float sc[3];
      {
        const unsigned sb = ldsb + MISC + (unsigned)(w ^ 2) * 384u + r2 * 8u;
#pragma unroll
        for (int q = 0; q < 3; ++q) {
          const f32x2 o = *(const AS3 f32x2*)(size_t)(sb + q * 128u);
          const float mn = fmaxf(mrun[q], o[0]);
          const float a0 = __builtin_amdgcn_exp2f((mrun[q] - mn) * SM_C), a1 = __builtin_amdgcn_exp2f((o[0] - mn) * SM_C);
          const float l = lsum[q] * a0 + o[1] * a1;
          sc[q] = a0 / l;
        }
      }
      const unsigned xreg = pr == 0 ? WAREA : 0u;
      const unsigned xw = ldsb + xreg + (unsigned)w * 18432u + ln * 16u;
      const unsigned xr = ldsb + xreg + (unsigned)(w ^ 2) * 18432u + ln * 16u;
      if (kh) {
#pragma unroll
        for (int d = 0; d < 6; ++d)
#pragma unroll
          for (int q = 0; q < 3; ++q) { stf4(xw + (unsigned)(d * 3 + q) * 1024u, O[d][q] * sc[q]); O[d][q] = O[6 + d][q] * sc[q]; }
      } else {
#pragma unroll
        for (int d = 0; d < 6; ++d)
#pragma unroll
          for (int q = 0; q < 3; ++q) { stf4(xw + (unsigned)(d * 3 + q) * 1024u, O[6 + d][q] * sc[q]); O[d][q] = O[d][q] * sc[q]; }
      }
      bar();
#pragma unroll
      for (int d = 0; d < 6; ++d)
#pragma unroll
        for (int q = 0; q < 3; ++q) O[d][q] += ldf4(xr + (unsigned)(d * 3 + q) * 1024u);
      asm volatile("s_waitcnt lgkmcnt(0)" ::: "memory");
#pragma unroll
      for (int q = 0; q < 3; ++q)
#pragma unroll
        for (int s = 0; s < 3; ++s) st128(xr + (unsigned)(q * 3 + s) * 1024u, pack8(O[2 * s][q], O[2 * s + 1][q]));
      bar();
      STAMP(100 + pr);
      bar();
    }
    {
      const unsigned l4 = lane_id();
      const unsigned fo = ((l4 >> 4) & 3u) << 1;
      const unsigned voO = (l4 >> 3) * 1280u + (((l4 & 7u) ^ fo) << 4);
      const unsigned wob = (unsigned)(h0 + 1) * 192u * 1280u + (unsigned)lw * 48u * 1280u;
      auto issue = [&](int c) {
        const unsigned la = ldsb + obuf_addr(c) + (unsigned)lw * 6144u;
#pragma unroll
        for (int i = 0; i < 6; ++i) dma16(rfl(la + 1024u * i), voO, srdO, rfl(wob + (unsigned)i * 10240u + (unsigned)c * 128u));
      };
      issue(2); issue(3); issue(4); issue(5);
#pragma unroll
      for (int c = 0; c < 10; ++c) {
        if (c >= 2) {
          const int last = (c + 4 < 9 ? c + 4 : 9);
          if (last - c == 4) waitvm<24>(); else if (last - c == 3) waitvm<18>(); else if (last - c == 2) waitvm<12>(); else if (last - c == 1) waitvm<6>(); else waitvm<0>();
        }
        bar();
        if (c >= 1 && c + 5 <= 9) issue(c + 5);
      }
    }
  } else {
    const int hw = w - 4, rh = hw & 1, xh = hw >> 1;
    const __amdgpu_buffer_rsrc_t srdK = __builtin_amdgcn_make_buffer_rsrc((void*)kg, 0, KV_BYTES, 0x00020000);
    const __amdgpu_buffer_rsrc_t srdV = __builtin_amdgcn_make_buffer_rsrc((void*)vg, 0, KV_BYTES, 0x00020000);
    unsigned voK[3], voV[3];
#pragma unroll
    for (int i = 0; i < 3; ++i) {
      const unsigned p = 64u * i + lane;
      const unsigned row = p / 24u, c = p % 24u;
      voK[i] = (row + 16u * hw) * 384u + ((c ^ ((row >> 1) & 7u)) << 4);
      voV[i] = (row + 16u * hw) * 384u + ((c ^ (((row >> 1) & 3u) << 1)) << 4);
    }
    const unsigned ldsw = ldsb + 6144u * hw;
    auto issueKV = [&](int u) {
      const unsigned pos = ldsw + (unsigned)(u % 3) * UNIT;
      const unsigned go = kvbase + (unsigned)(u >> 1) * UNIT;
      if (u & 1) {
#pragma unroll
        for (int i = 0; i < 3; ++i) dma16nt(rfl(pos + 1024u * i), voV[i], srdV, rfl(go));
#pragma unroll
        for (int i = 0; i < 3; ++i) dma16nt(rfl(pos + 3072u + 1024u * i), voV[i], srdV, rfl(go + 3072u));
      } else {
#pragma unroll
        for (int i = 0; i < 3; ++i) dma16nt(rfl(pos + 1024u * i), voK[i], srdK, rfl(go));
#pragma unroll
        for (int i = 0; i < 3; ++i) dma16nt(rfl(pos + 3072u + 1024u * i), voK[i] ^ 64u, srdK, rfl(go + 3072u));
      }
    };
    const unsigned qq = r >> 2, pp = r & 3;
    const unsigned fw = ((qq >> 1) | (((unsigned)g & 1u) << 1)) << 1;
    unsigned wa[3];
#pragma unroll
    for (int s = 0; s < 3; ++s) {
      const unsigned sg = 3u * xh + s;
      wa[s] = 384u * (8u * g + qq) + ((((sg & 1u) * 4u + pp) ^ fw) << 4) + 128u * (sg >> 1);
    }
    unsigned E = rfl(__hip_atomic_load(epw, __ATOMIC_RELAXED, __HIP_MEMORY_SCOPE_AGENT));
    asm volatile("" : "+s"(E));
    f32x4 QA[3][2][3];
    auto zeroQA = [&]() {
#pragma unroll
      for (int s = 0; s < 3; ++s)
#pragma unroll
        for (int a = 0; a < 2; ++a)
#pragma unroll
          for (int q = 0; q < 3; ++q) QA[s][a][q] = f32x4{0.f, 0.f, 0.f, 0.f};
    };
    auto writeQ = [&]() {
      const unsigned qw = ldsb + QBUF + (unsigned)rh * 18432u + lane_id() * 16u;
#pragma unroll
      for (int q = 0; q < 3; ++q)
#pragma unroll
        for (int s = 0; s < 3; ++s)
          st128(qw + (unsigned)(q * 6 + 3 * xh + s) * 1024u, pack8(QA[s][0][q], QA[s][1][q]));
    };
    auto qstep = [&](unsigned wb, const bf16x8 (&xf)[3]) {
      bf16x8 af[6];
#pragma unroll
      for (int i = 0; i < 6; ++i) { const unsigned ad = wb + wa[i >> 1] + 8u * (i & 1); af[i] = cat(ldtr(ad), ldtr(ad + 1536u)); }
      SB();
#pragma unroll
      for (int i = 0; i < 6; ++i)
#pragma unroll
        for (int q = 0; q < 3; ++q) QA[i >> 1][i & 1][q] = mfma(af[i], xf[q], QA[i >> 1][i & 1][q]);
      SB();
    };
    zeroQA();
    {
      const unsigned fk = (r >> 1) & 7;
      const unsigned xa0 = 128u * (48u * rh + r) + (((unsigned)g ^ fk) << 4);
      const unsigned xa1 = 128u * (48u * rh + r) + (((4u + g) ^ fk) << 4);
      for (int c = 0; c < 10; ++c) {
        bar();
        STAMP(102 + c);
        if (c == 0) issueKV(0); else if (c == 3) issueKV(1); else if (c == 6) issueKV(2);
        const unsigned base = ldsb + ((c & 1) ? WAREA : QBUF);
        bf16x8 xf0[3], xf1[3];
#pragma unroll
        for (int q = 0; q < 3; ++q) { xf0[q] = ld128(base + 2u * SLOT + xa0 + 2048u * q); xf1[q] = ld128(base + 2u * SLOT + xa1 + 2048u * q); }
        qstep(base, xf0);
        qstep(base + SLOT, xf1);
      }
    }
    writeQ();
    zeroQA();
    {
      unsigned voW[3], voX0, voX1;
#pragma unroll
      for (int i = 0; i < 3; ++i) {
        const unsigned p = 64u * (3u * hw + i) + lane;
        const unsigned row = p / 24u, c = p % 24u;
        voW[i] = row * 12288u + ((c ^ ((((row >> 1) & 1u) | (((row >> 3) & 1u) << 1)) << 1)) << 4);
      }
      {
        const unsigned p0 = 96u * hw + lane, p1 = p0 + 64u;
        voX0 = (p0 >> 2) * 1280u + (((p0 & 3u) ^ ((0u - (p0 >> 4)) & 3u)) << 4);
        voX1 = (p1 >> 2) * 1280u + (((p1 & 3u) ^ ((0u - (p1 >> 4)) & 3u)) << 4);
      }
      const unsigned xra = 64u * (48u * rh + r) + ((((unsigned)g) ^ ((0u - ((unsigned)r >> 2)) & 3u)) << 4);
      const unsigned wq1 = (unsigned)(h0 + 1) * 384u;
      int prev = 12;
      int jn = 0, jc = 0;
      for (int h = 0; h < 32; ++h) {
        wait_vm_dyn(prev);
        STAMP(12 + h);
        bar();
        int cnt = 0;
        const int hm = h % 3;
        if (hm != 2 && jn < 20) {
          const unsigned wbuf = ldsb + WAREA + (unsigned)(jn & 1) * 18432u;
          const unsigned so = (unsigned)(32 * jn) * 12288u + wq1;
#pragma unroll
          for (int i = 0; i < 3; ++i) dma16(rfl(wbuf + 3072u * hw + 1024u * i), voW[i], srdW, rfl(so));
          const unsigned xo = xbase + (unsigned)jn * 64u;
          dma16(rfl(wbuf + SLOT + 1536u * hw), voX0, srdX, rfl(xo));
          if (lane < 32) dma16(rfl(wbuf + SLOT + 1536u * hw + 1024u), voX1, srdX, rfl(xo));
          ++jn; cnt += 5;
        }
        if (h >= 1) { issueKV(h + 2); }
        cnt += 6;
        if (h >= 2 && hm != 1 && jc < 20) {
          const unsigned wbuf = ldsb + WAREA + (unsigned)(jc & 1) * 18432u;
          bf16x8 xf[3];
#pragma unroll
          for (int q = 0; q < 3; ++q) xf[q] = ld128(wbuf + SLOT + xra + 1024u * q);
          qstep(wbuf, xf);
          ++jc;
        }
        prev = cnt;
      }
    }
    bar();
    issueKV(34);
    bar(); bar();
    bf16x8 Of[6][3];
    auto readO = [&](unsigned xreg) {
#pragma unroll
      for (int k2 = 0; k2 < 2; ++k2) {
        const unsigned src = ldsb + xreg + (unsigned)(rh + 2 * (1 - k2)) * 18432u + lane_id() * 16u;
#pragma unroll
        for (int q = 0; q < 3; ++q)
#pragma unroll
          for (int s = 0; s < 3; ++s) Of[3 * k2 + s][q] = ld128(src + (unsigned)(q * 3 + s) * 1024u);
      }
    };
    readO(WAREA);
    bar();
    writeQ();
    const unsigned l3 = lane_id(), g3 = l3 >> 4, qq3 = (l3 & 15u) >> 2, pp3 = l3 & 3u;
    const unsigned fv = (2u * (g3 & 1) + (qq3 >> 1)) << 1;
    unsigned oa[2];
#pragma unroll
    for (int nb = 0; nb < 2; ++nb) oa[nb] = ldsb + 128u * (4u * g3 + qq3) + (((4u * xh + 2u * nb + (pp3 >> 1)) ^ fv) << 4) + 8u * (pp3 & 1);
    const unsigned fo = ((l3 >> 4) & 3u) << 1;
    const unsigned voO = (l3 >> 3) * 1280u + (((l3 & 7u) ^ fo) << 4);
    auto ohalf = [&](unsigned cb, int nb, f32x4 (&acc)[3]) {
      bf16x8 af[6];
#pragma unroll
      for (int ks = 0; ks < 6; ++ks) { const unsigned ad = oa[nb] + cb + 4096u * ks; af[ks] = cat(ldtr(ad), ldtr(ad + 2048u)); }
      SB();
#pragma unroll
      for (int ks = 0; ks < 6; ++ks)
#pragma unroll
        for (int q = 0; q < 3; ++q) acc[q] = mfma(af[ks], Of[ks][q], acc[q]);
      SB();
    };
    bf16x4 PP[10][2][3];
    {
      const unsigned wob0 = (unsigned)h0 * 192u * 1280u + (unsigned)hw * 48u * 1280u;
      auto issueO = [&](unsigned buf, unsigned wob, int c) {
#pragma unroll
        for (int i = 0; i < 6; ++i) dma16(rfl(ldsb + buf + 6144u * hw + 1024u * i), voO, srdO, rfl(wob + (unsigned)i * 10240u + (unsigned)c * 128u));
      };
      int prev = 12;
      for (int h = 0; h < 32; ++h) {
        wait_vm_dyn(prev);
        STAMP(44 + h);
        bar();
        int cnt = 0;
        if ((h & 1) == 0) {
          const int c = h >> 1;
          if (c < 10) { issueO(WAREA + (unsigned)(c & 1) * UNIT, wob0, c); cnt += 6; }
          else if (c == 12 || c == 13) { issueO(WAREA + (unsigned)(c & 1) * UNIT, wob0 + 192u * 1280u, c - 12); cnt += 6; }
        }
        if (h >= 1 && 34 + h < 64) issueKV(34 + h);
        if (34 + h < 64) cnt += 6;
        if ((h & 1) == 1 && h >= 3 && h <= 21) {
          const int c = (h - 3) >> 1;
          const unsigned cb = WAREA + (unsigned)(c & 1) * UNIT;
#pragma unroll
          for (int nb = 0; nb < 2; ++nb) {
            f32x4 acc[3] = {f32x4{0.f, 0.f, 0.f, 0.f}, f32x4{0.f, 0.f, 0.f, 0.f}, f32x4{0.f, 0.f, 0.f, 0.f}};
            ohalf(cb, nb, acc);
            bf16x4 pk[3];
#pragma unroll
            for (int q = 0; q < 3; ++q) pk[q] = __builtin_convertvector(acc[q], bf16x4);
#define PPCASE(K) if (c == K) { asm volatile("" ::: "memory"); PP[K][nb][0] = pk[0]; PP[K][nb][1] = pk[1]; PP[K][nb][2] = pk[2]; }
            PPCASE(0) else PPCASE(1) else PPCASE(2) else PPCASE(3) else PPCASE(4) else PPCASE(5) else PPCASE(6) else PPCASE(7) else PPCASE(8) else PPCASE(9)
#undef PPCASE
          }
        }
        prev = cnt;
      }
    }
    waitvm<0>();
    bar(); bar(); bar();
    readO(0u);
    bar();
    {
      const unsigned pbase = (unsigned)bid * PART_BYTES + (unsigned)hw * 30720u;
      const unsigned l16 = lane_id() * 16u;
#pragma unroll
      for (int c = 0; c < 10; ++c) {
        bar();
        STAMP(114 + c);
        const unsigned cb = obuf_addr(c);
        f32x4 acc[2][3];
#pragma unroll
        for (int nb = 0; nb < 2; ++nb) {
#pragma unroll
          for (int q = 0; q < 3; ++q) acc[nb][q] = unpack4(PP[c][nb][q]);
          ohalf(cb, nb, acc[nb]);
        }
#pragma unroll
        for (int q = 0; q < 3; ++q)
          __builtin_amdgcn_raw_buffer_store_b128(__builtin_bit_cast(u32x4, pack8(acc[0][q], acc[1][q])), srdP, l16,
                                                 pbase + (unsigned)(c * 3 + q) * 1024u, 16  );
      }
      waitvm<0>();
    }
    bar();
    const unsigned tag = E + 1u;
    if (hw == 0) {
      const unsigned l5 = lane_id();
      if (l5 == 0) __hip_atomic_store(epw + 32 * (1 + jj), tag, __ATOMIC_RELAXED, __HIP_MEMORY_SCOPE_AGENT);
      for (unsigned spin = 0; spin < (1u << 22); ++spin) {
        const unsigned v = __hip_atomic_load(epw + 32 * (1 + (l5 & 15)), __ATOMIC_RELAXED, __HIP_MEMORY_SCOPE_AGENT);
        if (__all(v == tag)) break;
        __builtin_amdgcn_s_sleep(1);
      }
      if (jj == 0 && l5 == 0) __hip_atomic_store(epw, tag, __ATOMIC_RELAXED, __HIP_MEMORY_SCOPE_AGENT);
      __builtin_amdgcn_fence(__ATOMIC_ACQUIRE, "agent");
      waitvm<0>();
    }
  }
  if (w < 4) bar();
  bar();
  STAMP(126);
  {
    const int piece = jj * 8 + w;
    const unsigned l6 = lane_id(), r6 = l6 & 15u, g6 = l6 >> 4;
    if (piece < 120) {
      float s[8];
#pragma unroll
      for (int i = 0; i < 8; ++i) s[i] = 0.f;
      u32x4 v[16];
#pragma unroll
      for (int k = 0; k < 16; ++k) {
        const unsigned obid = (unsigned)(((b * 2 + (k & 1)) << 3) | (k >> 1));
        v[k] = bload(srdP, l6 * 16u, obid * PART_BYTES + (unsigned)piece * 1024u);
      }
#pragma unroll
      for (int k = 0; k < 16; ++k)
#pragma unroll
        for (int i = 0; i < 4; ++i) { s[2 * i] += __uint_as_float(v[k][i] << 16); s[2 * i + 1] += __uint_as_float(v[k][i] & 0xffff0000u); }
      const int hp = piece / 30, rem = piece % 30, c = rem / 3, q = rem % 3;
      const int row = 48 * (hp & 1) + 16 * q + (int)r6, n0 = 64 * c + 32 * (hp >> 1) + 4 * (int)g6;
      __bf16* op = (__bf16*)outg + ((size_t)b * SQ + row) * DM + n0;
      *(bf16x4*)op = __builtin_convertvector((f32x4{s[0], s[1], s[2], s[3]}), bf16x4);
      *(bf16x4*)(op + 16) = __builtin_convertvector((f32x4{s[4], s[5], s[6], s[7]}), bf16x4);
    }
  }
  STAMP(127);
  STAMP_DUMP();
}

extern "C" void kernel_launch(void* const* d_in, const int* in_sizes, int n_in, void* d_out, int out_size, void* d_ws, size_t ws_size,
                              hipStream_t stream) {
  (void)in_sizes; (void)n_in; (void)out_size; (void)ws_size;
  (void)hipFuncSetAttribute((const void*)mha_kernel, hipFuncAttributeMaxDynamicSharedMemorySize, LDS_BYTES);
  mha_kernel<<<256, 512, LDS_BYTES, stream>>>(d_in[0], d_in[1], d_in[2], d_in[3], d_in[4], d_out, d_ws);
}
```

```cpp
#include <hip/hip_runtime.h>
#include <hip/hip_bf16.h>
#include <stdint.h>
#define NOPRIO 1

typedef __bf16 bf16x8 __attribute__((ext_vector_type(8)));
typedef __bf16 bf16x4 __attribute__((ext_vector_type(4)));
typedef float f32x4 __attribute__((ext_vector_type(4)));
typedef float f32x2 __attribute__((ext_vector_type(2)));
typedef unsigned u32x4 __attribute__((ext_vector_type(4)));
typedef unsigned u32x2 __attribute__((ext_vector_type(2)));
typedef unsigned u32x10 __attribute__((ext_vector_type(10)));
#define AS3 __attribute__((address_space(3)))
#define AS1 __attribute__((address_space(1)))
#define DI __device__ __forceinline__
#define SB() __builtin_amdgcn_sched_barrier(0)

constexpr int NB = 16, SQ = 96, DM = 640, NH = 32, SK = 1024, DH = 192;
constexpr unsigned SLOT = 12288u, UNIT = 24576u;
constexpr unsigned WAREA = 3u * UNIT;
constexpr unsigned QBUF = 5u * UNIT;
constexpr unsigned MISC = QBUF + 36864u;
constexpr unsigned LDS_BYTES = 163840u;
constexpr unsigned KV_BYTES = (unsigned)NB * NH * SK * DH * 2u;
constexpr unsigned W_BYTES = (unsigned)DM * NH * DH * 2u;
constexpr unsigned X_BYTES = (unsigned)NB * SQ * DM * 2u;
constexpr unsigned PART_BYTES = 122880u;
constexpr unsigned WS_FLAGS = 0u;
constexpr unsigned WS_PART = 65536u;
constexpr float SM_C = 0.07216878364870322f * 1.4426950408889634f;
constexpr float RESC_THR = 6.0f / SM_C;

DI void dma16(unsigned ldsaddr, unsigned voff, __amdgpu_buffer_rsrc_t srd, unsigned soff) {
  asm volatile("s_mov_b32 m0, %0\n\ts_nop 0\n\tbuffer_load_dwordx4 %1, %2, %3 offen lds" :: "s"(ldsaddr), "v"(voff), "s"(srd), "s"(soff) : "memory", "m0");
}
DI void dma16nt(unsigned ldsaddr, unsigned voff, __amdgpu_buffer_rsrc_t srd, unsigned soff) {
  asm volatile("s_mov_b32 m0, %0\n\ts_nop 0\n\tbuffer_load_dwordx4 %1, %2, %3 offen nt lds" :: "s"(ldsaddr), "v"(voff), "s"(srd), "s"(soff) : "memory", "m0");
}
template <int N> DI void waitvm() { asm volatile("s_waitcnt vmcnt(%0)" :: "i"(N) : "memory"); }
DI void wait_vm_dyn(int n) {
  if (n >= 12) waitvm<12>(); else if (n == 11) waitvm<11>(); else if (n >= 6) waitvm<6>(); else if (n == 5) waitvm<5>(); else waitvm<0>();
}
DI void bar() { asm volatile("s_waitcnt lgkmcnt(0)\n\ts_barrier" ::: "memory"); }
DI unsigned lane_id() { unsigned l; asm volatile("v_mbcnt_lo_u32_b32 %0, -1, 0\n\tv_mbcnt_hi_u32_b32 %0, -1, %0" : "=v"(l)); return l; }
DI unsigned rfl(unsigned x) { return __builtin_amdgcn_readfirstlane(x); }
DI bf16x8 ld128(unsigned a) { return *(const AS3 bf16x8*)(size_t)a; }
DI f32x4 ldf4(unsigned a) { return *(const AS3 f32x4*)(size_t)a; }
DI void st128(unsigned a, bf16x8 v) { *(AS3 bf16x8*)(size_t)a = v; }
DI void stu4(unsigned a, u32x4 v) { *(AS3 u32x4*)(size_t)a = v; }
DI void stf4(unsigned a, f32x4 v) { *(AS3 f32x4*)(size_t)a = v; }
DI bf16x4 ldtr(unsigned a) { return __builtin_amdgcn_ds_read_tr16_b64_v4bf16((AS3 bf16x4*)(size_t)a); }
DI bf16x8 cat(bf16x4 lo, bf16x4 hi) { return __builtin_shufflevector(lo, hi, 0, 1, 2, 3, 4, 5, 6, 7); }
DI f32x4 mfma(bf16x8 a, bf16x8 b, f32x4 c) { return __builtin_amdgcn_mfma_f32_16x16x32_bf16(a, b, c, 0, 0, 0); }
DI u32x4 bload(__amdgpu_buffer_rsrc_t srd, unsigned voff, unsigned soff) {
  return __builtin_amdgcn_raw_buffer_load_b128(srd, voff, soff, 0);
}
DI bf16x8 pack8(f32x4 a, f32x4 b) {
  bf16x4 lo = __builtin_convertvector(a, bf16x4), hi = __builtin_convertvector(b, bf16x4);
  return cat(lo, hi);
}
DI f32x4 unpack4(bf16x4 v) {
  const u32x2 u = __builtin_bit_cast(u32x2, v);
  return f32x4{__uint_as_float(u[0] << 16), __uint_as_float(u[0] & 0xffff0000u), __uint_as_float(u[1] << 16), __uint_as_float(u[1] & 0xffff0000u)};
}
DI float max3f(float a, float b, float c) { float r; asm("v_max3_f32 %0, %1, %2, %3" : "=v"(r) : "v"(a), "v"(b), "v"(c)); return r; }
DI float max2f(float a, float b) { float r; asm("v_max_f32_e32 %0, %1, %2" : "=v"(r) : "v"(a), "v"(b)); return r; }
DI float xmax_g(float v) {
  auto a = __builtin_amdgcn_permlane16_swap(__float_as_uint(v), __float_as_uint(v), false, false);
  v = fmaxf(__uint_as_float(a[0]), __uint_as_float(a[1]));
  auto b = __builtin_amdgcn_permlane32_swap(__float_as_uint(v), __float_as_uint(v), false, false);
  return fmaxf(__uint_as_float(b[0]), __uint_as_float(b[1]));
}
DI float xsum_g(float v) {
  auto a = __builtin_amdgcn_permlane16_swap(__float_as_uint(v), __float_as_uint(v), false, false);
  v = __uint_as_float(a[0]) + __uint_as_float(a[1]);
  auto b = __builtin_amdgcn_permlane32_swap(__float_as_uint(v), __float_as_uint(v), false, false);
  return __uint_as_float(b[0]) + __uint_as_float(b[1]);
}
#define STAMP(i) do {} while (0)
#define STAMP_DUMP() do {} while (0)

__global__ __launch_bounds__(512, 2) void mha_kernel(const void* __restrict__ xg, const void* __restrict__ kg, const void* __restrict__ vg,
                                                     const void* __restrict__ wqg, const void* __restrict__ wog, void* __restrict__ outg,
                                                     void* __restrict__ ws) {
  extern __shared__ __attribute__((aligned(16))) char lds_raw[];
  const unsigned ldsb = (unsigned)(size_t)(AS3 char*)lds_raw;
  const int tid = threadIdx.x, lane = tid & 63;
  const int w = __builtin_amdgcn_readfirstlane(tid >> 6);
  const int r = lane & 15, g = lane >> 4;
  const int bid = blockIdx.x, xcd = bid & 7, idx = bid >> 3;
  const int b = idx >> 1, jj = 2 * xcd + (idx & 1), h0 = 2 * jj;
  const unsigned kvbase = (unsigned)(b * NH + h0) * (unsigned)(SK * DH * 2);
  const int lw = w & 3;
  STAMP(0);
  AS1 unsigned* const epw = (AS1 unsigned*)((char*)ws + WS_FLAGS) + (size_t)b * 17 * 32;
  const __amdgpu_buffer_rsrc_t srdP = __builtin_amdgcn_make_buffer_rsrc((char*)ws + WS_PART, 0, 256u * PART_BYTES, 0x00020000);
  const __amdgpu_buffer_rsrc_t srdW = __builtin_amdgcn_make_buffer_rsrc((void*)wqg, 0, W_BYTES, 0x00020000);
  const __amdgpu_buffer_rsrc_t srdX = __builtin_amdgcn_make_buffer_rsrc((void*)xg, 0, X_BYTES, 0x00020000);
  const __amdgpu_buffer_rsrc_t srdO = __builtin_amdgcn_make_buffer_rsrc((void*)wog, 0, W_BYTES, 0x00020000);
  const unsigned xbase = (unsigned)b * (unsigned)(SQ * DM * 2);
  auto obuf_addr = [&](int c) -> unsigned { const int k = c % 6; return k < 2 ? WAREA + (unsigned)k * UNIT : (k < 5 ? (unsigned)(k - 2) * UNIT : QBUF); };

  if (w < 4) {
    const int rh = w & 1, kh = w >> 1;
    {
      unsigned voW[3], voX[3];
#pragma unroll
      for (int i = 0; i < 3; ++i) {
        const unsigned p = 64u * (3u * lw + i) + lane;
        const unsigned row = p / 24u, c = p % 24u;
        voW[i] = row * 12288u + ((c ^ ((((row >> 1) & 1u) | (((row >> 3) & 1u) << 1)) << 1)) << 4);
        const unsigned rx = p >> 3, cx = p & 7u;
        voX[i] = rx * 1280u + ((cx ^ ((rx >> 1) & 7u)) << 4);
      }
      const unsigned stw = ldsb + 3072u * lw + lane * 16u;
      const unsigned wq0 = (unsigned)h0 * 384u;
      constexpr int QD = 5;
      u32x4 R[QD][9];
      auto ldc = [&](int s, int c) {
        const unsigned wo = (unsigned)(64 * c) * 12288u + wq0;
#pragma unroll
        for (int i = 0; i < 3; ++i) R[s][i] = bload(srdW, voW[i], wo);
#pragma unroll
        for (int i = 0; i < 3; ++i) R[s][3 + i] = bload(srdW, voW[i], wo + 32u * 12288u);
#pragma unroll
        for (int i = 0; i < 3; ++i) R[s][6 + i] = bload(srdX, voX[i], xbase + (unsigned)c * 128u);
      };
      auto stc = [&](int s, int c) {
        const unsigned base = stw + ((c & 1) ? WAREA : QBUF);
#pragma unroll
        for (int j = 0; j < 3; ++j)
#pragma unroll
          for (int i = 0; i < 3; ++i) stu4(base + (unsigned)j * SLOT + 1024u * i, R[s][3 * j + i]);
      };
#pragma unroll
      for (int c = 0; c < QD; ++c) ldc(c, c);
      SB();
      stc(0, 0); ldc(0, QD);
      bar();
#pragma unroll
      for (int c = 0; c < 9; ++c) {
        stc((c + 1) % QD, c + 1);
        if (c + 1 + QD <= 9) ldc((c + 1) % QD, c + 1 + QD);
        bar();
      }
    }
    STAMP(1);
    const unsigned ln = lane_id();
    const unsigned r2 = ln & 15u, g2 = ln >> 4;
    const unsigned fk = (r2 >> 1) & 7;
    const unsigned ka0 = ldsb + kh * SLOT + 384u * r2 + ((g2 ^ fk) << 4);
    const unsigned ka1 = ldsb + kh * SLOT + 384u * r2 + (((4u + g2) ^ fk) << 4);
    const unsigned qa = ldsb + QBUF + (unsigned)rh * 18432u + ln * 16u;
    const unsigned qq = r2 >> 2, pp = r2 & 3;
    const unsigned fv = (2u * (g2 & 1) + (qq >> 1)) << 1;
    unsigned va[4];
#pragma unroll
    for (int d = 0; d < 4; ++d) va[d] = ldsb + kh * SLOT + 384u * (4u * g2 + qq) + (((2u * d + (pp >> 1)) ^ fv) << 4) + 8u * (pp & 1);

#pragma unroll
    for (int pr = 0; pr < 2; ++pr) {
      f32x4 O[12][3];
#pragma unroll
      for (int d = 0; d < 12; ++d)
#pragma unroll
        for (int q = 0; q < 3; ++q) O[d][q] = f32x4{0.f, 0.f, 0.f, 0.f};
      float mrun[3] = {-INFINITY, -INFINITY, -INFINITY};
      f32x2 lsum2[3] = {f32x2{0.f, 0.f}, f32x2{0.f, 0.f}, f32x2{0.f, 0.f}};
      unsigned kpos = (unsigned)((32 * pr) % 3) * UNIT;
      for (int t = 0; t < 16; ++t) {
        const unsigned vpos = kpos == 2u * UNIT ? 0u : kpos + UNIT;
        f32x4 S[2][3];
        {
          bf16x8 kf[3][2], qf[3][3];
          auto ldq = [&](int s) {
#pragma unroll
            for (int q = 0; q < 3; ++q) qf[s % 3][q] = ld128(qa + (unsigned)(q * 6 + s) * 1024u);
          };
          const unsigned k0 = ka0 + kpos, k1 = ka1 + kpos;
          auto ldk = [&](int s) {
            const unsigned kb = ((s & 1) ? k1 : k0) + 128u * (s >> 1);
            kf[s % 3][0] = ld128(kb); kf[s % 3][1] = ld128(kb + 6144u);
          };
          if (t > 0) { ldq(0); ldq(1); }
          asm volatile("s_barrier" ::: "memory");
          STAMP(2 + 3 * (pr * 16 + t));
          if (t == 0) { ldq(0); ldq(1); }
          ldk(0); ldk(1);
#pragma unroll
          for (int q = 0; q < 3; ++q) { S[0][q] = f32x4{0.f, 0.f, 0.f, 0.f}; S[1][q] = f32x4{0.f, 0.f, 0.f, 0.f}; }
#pragma unroll
          for (int s = 0; s < 6; ++s) {
            SB();
            if (s + 2 < 6) { ldk(s + 2); ldq(s + 2); }
            SB();
#pragma unroll
            for (int q = 0; q < 3; ++q) {
              S[0][q] = mfma(kf[s % 3][0], qf[s % 3][q], S[0][q]);
              S[1][q] = mfma(kf[s % 3][1], qf[s % 3][q], S[1][q]);
            }
          }
          SB();
        }
        bar();
        STAMP(4 + 3 * (pr * 16 + t));
        bf16x8 vf[7];
        auto ldv = [&](int d) {
          const unsigned a = va[d & 3] + vpos + 128u * (d >> 2);
          vf[d % 7] = cat(ldtr(a), ldtr(a + 6144u));
        };
        ldv(0); ldv(1); ldv(2); ldv(3); ldv(4); ldv(5);
        SB();
        float mx[3];
        bool need = false;
#pragma unroll
        for (int q = 0; q < 3; ++q) {
          mx[q] = max3f(max3f(S[0][q][0], S[0][q][1], S[0][q][2]), max3f(S[0][q][3], S[1][q][0], S[1][q][1]), max2f(S[1][q][2], S[1][q][3]));
          need |= (mx[q] > mrun[q] + RESC_THR);
        }
        if (__any(need)) {
#pragma unroll
          for (int q = 0; q < 3; ++q) {
            const float mn = fmaxf(mrun[q], xmax_g(mx[q]));
            const float al = __builtin_amdgcn_exp2f((mrun[q] - mn) * SM_C);
            mrun[q] = mn;
            lsum2[q] *= al;
#pragma unroll
            for (int d = 0; d < 12; ++d) O[d][q] *= al;
          }
        }
        bf16x8 pf[3];
#pragma unroll
        for (int q = 0; q < 3; ++q) {
          const float nm = -mrun[q] * SM_C;
          const f32x2 nm2 = {nm, nm}, c2 = {SM_C, SM_C};
          f32x2 e[4];
#pragma unroll
          for (int i = 0; i < 4; ++i) {
            const f32x2 sv = {S[i >> 1][q][2 * (i & 1)], S[i >> 1][q][2 * (i & 1) + 1]};
            const f32x2 a = sv * c2 + nm2;
            e[i] = f32x2{__builtin_amdgcn_exp2f(a[0]), __builtin_amdgcn_exp2f(a[1])};
          }
          lsum2[q] += (e[0] + e[1]) + (e[2] + e[3]);
          pf[q] = pack8(f32x4{e[0][0], e[0][1], e[1][0], e[1][1]}, f32x4{e[2][0], e[2][1], e[3][0], e[3][1]});
        }
        STAMP(3 + 3 * (pr * 16 + t));
#pragma unroll
        for (int d = 0; d < 12; ++d) {
          SB();
          if (d + 6 < 12) ldv(d + 6);
          SB();
#pragma unroll
          for (int q = 0; q < 3; ++q) O[d][q] = mfma(vf[d % 7], pf[q], O[d][q]);
        }
        SB();
        asm volatile("s_waitcnt lgkmcnt(0)" ::: "memory");
        kpos = vpos == 2u * UNIT ? 0u : vpos + UNIT;
      }
      float lsum[3];
      {
        const unsigned sa = ldsb + MISC + (unsigned)w * 384u + r2 * 8u;
#pragma unroll
        for (int q = 0; q < 3; ++q) {
          lsum[q] = xsum_g(lsum2[q][0] + lsum2[q][1]);
          if (g2 == 0) *(AS3 f32x2*)(size_t)(sa + q * 128u) = f32x2{mrun[q], lsum[q]};
        }
      }
      bar();
      float sc[3];
      {
        const unsigned sb = ldsb + MISC + (unsigned)(w ^ 2) * 384u + r2 * 8u;
#pragma unroll
        for (int q = 0; q < 3; ++q) {
          const f32x2 o = *(const AS3 f32x2*)(size_t)(sb + q * 128u);
          const float mn = fmaxf(mrun[q], o[0]);
          const float a0 = __builtin_amdgcn_exp2f((mrun[q] - mn) * SM_C), a1 = __builtin_amdgcn_exp2f((o[0] - mn) * SM_C);
          const float l = lsum[q] * a0 + o[1] * a1;
          sc[q] = a0 / l;
        }
      }
      const unsigned xreg = pr == 0 ? WAREA : 0u;
      const unsigned xw = ldsb + xreg + (unsigned)w * 18432u + ln * 16u;
      const unsigned xr = ldsb + xreg + (unsigned)(w ^ 2) * 18432u + ln * 16u;
      if (kh) {
#pragma unroll
        for (int d = 0; d < 6; ++d)
#pragma unroll
          for (int q = 0; q < 3; ++q) { stf4(xw + (unsigned)(d * 3 + q) * 1024u, O[d][q] * sc[q]); O[d][q] = O[6 + d][q] * sc[q]; }
      } else {
#pragma unroll
        for (int d = 0; d < 6; ++d)
#pragma unroll
          for (int q = 0; q < 3; ++q) { stf4(xw + (unsigned)(d * 3 + q) * 1024u, O[6 + d][q] * sc[q]); O[d][q] = O[d][q] * sc[q]; }
      }
      bar();
#pragma unroll
      for (int d = 0; d < 6; ++d)
#pragma unroll
        for (int q = 0; q < 3; ++q) O[d][q] += ldf4(xr + (unsigned)(d * 3 + q) * 1024u);
      asm volatile("s_waitcnt lgkmcnt(0)" ::: "memory");
#pragma unroll
      for (int q = 0; q < 3; ++q)
#pragma unroll
        for (int s = 0; s < 3; ++s) st128(xr + (unsigned)(q * 3 + s) * 1024u, pack8(O[2 * s][q], O[2 * s + 1][q]));
      bar();
      STAMP(100 + pr);
      bar();
    }
    {
      const unsigned l4 = lane_id();
      const unsigned fo = ((l4 >> 4) & 3u) << 1;
      const unsigned voO = (l4 >> 3) * 1280u + (((l4 & 7u) ^ fo) << 4);
      const unsigned wob = (unsigned)(h0 + 1) * 192u * 1280u + (unsigned)lw * 48u * 1280u;
      auto issue = [&](int c) {
        const unsigned la = ldsb + obuf_addr(c) + (unsigned)lw * 6144u;
#pragma unroll
        for (int i = 0; i < 6; ++i) dma16(rfl(la + 1024u * i), voO, srdO, rfl(wob + (unsigned)i * 10240u + (unsigned)c * 128u));
      };
      issue(2); issue(3); issue(4); issue(5);
      for (int c = 0; c < 10; ++c) {
        if (c >= 2) {
          if (c <= 5) waitvm<24>(); else if (c == 6) waitvm<18>(); else if (c == 7) waitvm<12>(); else if (c == 8) waitvm<6>(); else waitvm<0>();
        }
        bar();
        if (c >= 1 && c + 5 <= 9) issue(c + 5);
      }
    }
  } else {
    const int hw = w - 4, rh = hw & 1, xh = hw >> 1;
    const __amdgpu_buffer_rsrc_t srdK = __builtin_amdgcn_make_buffer_rsrc((void*)kg, 0, KV_BYTES, 0x00020000);
    const __amdgpu_buffer_rsrc_t srdV = __builtin_amdgcn_make_buffer_rsrc((void*)vg, 0, KV_BYTES, 0x00020000);
    unsigned voK[3], voV[3];
#pragma unroll
    for (int i = 0; i < 3; ++i) {
      const unsigned p = 64u * i + lane;
      const unsigned row = p / 24u, c = p % 24u;
      voK[i] = (row + 16u * hw) * 384u + ((c ^ ((row >> 1) & 7u)) << 4);
      voV[i] = (row + 16u * hw) * 384u + ((c ^ (((row >> 1) & 3u) << 1)) << 4);
    }
    const unsigned ldsw = ldsb + 6144u * hw;
    auto issueKV = [&](int u) {
      const unsigned pos = ldsw + (unsigned)(u % 3) * UNIT;
      const unsigned go = kvbase + (unsigned)(u >> 1) * UNIT;
      if (u & 1) {
#pragma unroll
        for (int i = 0; i < 3; ++i) dma16nt(rfl(pos + 1024u * i), voV[i], srdV, rfl(go));
#pragma unroll
        for (int i = 0; i < 3; ++i) dma16nt(rfl(pos + 3072u + 1024u * i), voV[i], srdV, rfl(go + 3072u));
      } else {
#pragma unroll
        for (int i = 0; i < 3; ++i) dma16nt(rfl(pos + 1024u * i), voK[i], srdK, rfl(go));
#pragma unroll
        for (int i = 0; i < 3; ++i) dma16nt(rfl(pos + 3072u + 1024u * i), voK[i] ^ 64u, srdK, rfl(go + 3072u));
      }
    };
    const unsigned qq = r >> 2, pp = r & 3;
    const unsigned fw = ((qq >> 1) | (((unsigned)g & 1u) << 1)) << 1;
    unsigned wa[3];
#pragma unroll
    for (int s = 0; s < 3; ++s) {
      const unsigned sg = 3u * xh + s;
      wa[s] = 384u * (8u * g + qq) + ((((sg & 1u) * 4u + pp) ^ fw) << 4) + 128u * (sg >> 1);
    }
    unsigned E = rfl(__hip_atomic_load(epw, __ATOMIC_RELAXED, __HIP_MEMORY_SCOPE_AGENT));
    asm volatile("" : "+s"(E));
    f32x4 QA[3][2][3];
    auto zeroQA = [&]() {
#pragma unroll
      for (int s = 0; s < 3; ++s)
#pragma unroll
        for (int a = 0; a < 2; ++a)
#pragma unroll
          for (int q = 0; q < 3; ++q) QA[s][a][q] = f32x4{0.f, 0.f, 0.f, 0.f};
    };
    auto writeQ = [&]() {
      const unsigned qw = ldsb + QBUF + (unsigned)rh * 18432u + lane_id() * 16u;
#pragma unroll
      for (int q = 0; q < 3; ++q)
#pragma unroll
        for (int s = 0; s < 3; ++s)
          st128(qw + (unsigned)(q * 6 + 3 * xh + s) * 1024u, pack8(QA[s][0][q], QA[s][1][q]));
    };
    auto qload = [&](unsigned wb, bf16x8 (&af)[6]) {
#pragma unroll
      for (int i = 0; i < 6; ++i) { const unsigned ad = wb + wa[i >> 1] + 8u * (i & 1); af[i] = cat(ldtr(ad), ldtr(ad + 1536u)); }
    };
    auto qmma = [&](const bf16x8 (&af)[6], const bf16x8 (&xf)[3]) {
#pragma unroll
      for (int i = 0; i < 6; ++i)
#pragma unroll
        for (int q = 0; q < 3; ++q) QA[i >> 1][i & 1][q] = mfma(af[i], xf[q], QA[i >> 1][i & 1][q]);
    };
    zeroQA();
    {
      const unsigned fk = (r >> 1) & 7;
      const unsigned xa0 = 128u * (48u * rh + r) + (((unsigned)g ^ fk) << 4);
      const unsigned xa1 = 128u * (48u * rh + r) + (((4u + g) ^ fk) << 4);
      for (int c = 0; c < 10; ++c) {
        bar();
        STAMP(98 + c);
        if (c == 0) issueKV(0); else if (c == 3) issueKV(1); else if (c == 6) issueKV(2);
        const unsigned base = ldsb + ((c & 1) ? WAREA : QBUF);
        bf16x8 xf0[3], xf1[3], af0[6], af1[6];
#pragma unroll
        for (int q = 0; q < 3; ++q) xf0[q] = ld128(base + 2u * SLOT + xa0 + 2048u * q);
        qload(base, af0);
        SB();
#pragma unroll
        for (int q = 0; q < 3; ++q) xf1[q] = ld128(base + 2u * SLOT + xa1 + 2048u * q);
#pragma unroll
        for (int i = 0; i < 6; ++i) {
          SB();
          { const unsigned ad = base + SLOT + wa[i >> 1] + 8u * (i & 1); af1[i] = cat(ldtr(ad), ldtr(ad + 1536u)); }
          SB();
#pragma unroll
          for (int q = 0; q < 3; ++q) QA[i >> 1][i & 1][q] = mfma(af0[i], xf0[q], QA[i >> 1][i & 1][q]);
        }
        SB();
        qmma(af1, xf1);
        SB();
      }
    }
    writeQ();
    zeroQA();
    {
      unsigned voW[3], voX0, voX1;
#pragma unroll
      for (int i = 0; i < 3; ++i) {
        const unsigned p = 64u * (3u * hw + i) + lane;
        const unsigned row = p / 24u, c = p % 24u;
        voW[i] = row * 12288u + ((c ^ ((((row >> 1) & 1u) | (((row >> 3) & 1u) << 1)) << 1)) << 4);
      }
      {
        const unsigned p0 = 96u * hw + lane, p1 = p0 + 64u;
        voX0 = (p0 >> 2) * 1280u + (((p0 & 3u) ^ ((0u - (p0 >> 4)) & 3u)) << 4);
        voX1 = (p1 >> 2) * 1280u + (((p1 & 3u) ^ ((0u - (p1 >> 4)) & 3u)) << 4);
      }
      const unsigned xra = 64u * (48u * rh + r) + ((((unsigned)g) ^ ((0u - ((unsigned)r >> 2)) & 3u)) << 4);
      const unsigned wq1 = (unsigned)(h0 + 1) * 384u;
      int prev = 12;
      int jn = 0, jc = 0;
      for (int h = 0; h < 32; ++h) {
        wait_vm_dyn(prev);
        STAMP(1 + h);
        bar();
        int cnt = 0;
        const int hm = h % 3;
        const bool docomp = (h >= 2 && hm != 1 && jc < 20);
        bf16x8 xf[3], af[6];
        if (docomp) {
          const unsigned wbuf = ldsb + WAREA + (unsigned)(jc & 1) * 18432u;
#pragma unroll
          for (int q = 0; q < 3; ++q) xf[q] = ld128(wbuf + SLOT + xra + 1024u * q);
          qload(wbuf, af);
        }
        SB();
        if (hm != 2 && jn < 20) {
          const unsigned wbuf = ldsb + WAREA + (unsigned)(jn & 1) * 18432u;
          const unsigned so = (unsigned)(32 * jn) * 12288u + wq1;
#pragma unroll
          for (int i = 0; i < 3; ++i) dma16(rfl(wbuf + 3072u * hw + 1024u * i), voW[i], srdW, rfl(so));
          const unsigned xo = xbase + (unsigned)jn * 64u;
          dma16(rfl(wbuf + SLOT + 1536u * hw), voX0, srdX, rfl(xo));
          if (lane < 32) dma16(rfl(wbuf + SLOT + 1536u * hw + 1024u), voX1, srdX, rfl(xo));
          ++jn; cnt += 5;
        }
        if (h >= 1) { issueKV(h + 2); }
        cnt += 6;
        SB();
        if (docomp) { qmma(af, xf); ++jc; }
        SB();
        prev = cnt;
      }
    }
    bar();
    issueKV(34);
    bar(); bar();
    bf16x8 Of[6][3];
    auto readO = [&](unsigned xreg) {
#pragma unroll
      for (int k2 = 0; k2 < 2; ++k2) {
        const unsigned src = ldsb + xreg + (unsigned)(rh + 2 * (1 - k2)) * 18432u + lane_id() * 16u;
#pragma unroll
        for (int q = 0; q < 3; ++q)
#pragma unroll
          for (int s = 0; s < 3; ++s) Of[3 * k2 + s][q] = ld128(src + (unsigned)(q * 3 + s) * 1024u);
      }
    };
    readO(WAREA);
    bar();
    writeQ();
    const unsigned l3 = lane_id(), g3 = l3 >> 4, qq3 = (l3 & 15u) >> 2, pp3 = l3 & 3u;
    const unsigned fv = (2u * (g3 & 1) + (qq3 >> 1)) << 1;
    unsigned oa[2];
#pragma unroll
    for (int nb = 0; nb < 2; ++nb) oa[nb] = ldsb + 128u * (4u * g3 + qq3) + (((4u * xh + 2u * nb + (pp3 >> 1)) ^ fv) << 4) + 8u * (pp3 & 1);
    const unsigned fo = ((l3 >> 4) & 3u) << 1;
    const unsigned voO = (l3 >> 3) * 1280u + (((l3 & 7u) ^ fo) << 4);
    auto oload = [&](unsigned cb, int nb, bf16x8 (&af)[6]) {
#pragma unroll
      for (int ks = 0; ks < 6; ++ks) { const unsigned ad = oa[nb] + cb + 4096u * ks; af[ks] = cat(ldtr(ad), ldtr(ad + 2048u)); }
    };
    auto omma = [&](const bf16x8 (&af)[6], f32x4 (&acc)[3]) {
#pragma unroll
      for (int ks = 0; ks < 6; ++ks)
#pragma unroll
        for (int q = 0; q < 3; ++q) acc[q] = mfma(af[ks], Of[ks][q], acc[q]);
    };
    u32x10 PP[2][3][2];
#pragma unroll
    for (int nb = 0; nb < 2; ++nb)
#pragma unroll
      for (int q = 0; q < 3; ++q)
#pragma unroll
        for (int d = 0; d < 2; ++d)
#pragma unroll
          for (int c = 0; c < 10; ++c) PP[nb][q][d][c] = 0u;
    {
      const unsigned wob0 = (unsigned)h0 * 192u * 1280u + (unsigned)hw * 48u * 1280u;
      auto half = [&](int h, int nb, int cc, bool comp, int ci, int prev) -> int {
        wait_vm_dyn(prev);
        STAMP(33 + h);
        bar();
        bf16x8 af[6];
        if (comp) oload(WAREA + (unsigned)(cc & 1) * UNIT, nb, af);
        SB();
        int cnt = 0;
        if (ci >= 0) {
          const unsigned wob = wob0 + (ci >= 10 ? 192u * 1280u + (unsigned)(ci - 10) * 128u : (unsigned)ci * 128u);
#pragma unroll
          for (int i = 0; i < 6; ++i) dma16(rfl(ldsb + WAREA + (unsigned)(ci & 1) * UNIT + 6144u * hw + 1024u * i), voO, srdO, rfl(wob + (unsigned)i * 10240u));
          cnt += 6;
        }
        if (h >= 1 && 34 + h < 64) issueKV(34 + h);
        if (34 + h < 64) cnt += 6;
        SB();
        if (comp) {
          f32x4 acc[3] = {f32x4{0.f, 0.f, 0.f, 0.f}, f32x4{0.f, 0.f, 0.f, 0.f}, f32x4{0.f, 0.f, 0.f, 0.f}};
          omma(af, acc);
#pragma unroll
          for (int q = 0; q < 3; ++q) {
            const u32x2 pk = __builtin_bit_cast(u32x2, __builtin_convertvector(acc[q], bf16x4));
            PP[nb][q][0][cc] = pk[0]; PP[nb][q][1][cc] = pk[1];
          }
        }
        SB();
        STAMP(65 + h);
        return cnt;
      };
      int prev = 12;
      for (int i = 0; i < 16; ++i) {
        prev = half(2 * i, 1, i - 2, i >= 2 && i <= 11, -1, prev);
        prev = half(2 * i + 1, 0, i - 1, i >= 1 && i <= 10, i < 12 ? i : -1, prev);
      }
    }
    waitvm<0>();
    bar(); bar(); bar();
    readO(0u);
    bar();
    {
      const unsigned pbase = (unsigned)bid * PART_BYTES + (unsigned)hw * 30720u;
      const unsigned l16 = lane_id() * 16u;
      for (int c = 0; c < 10; ++c) {
        bar();
        STAMP(108 + c);
        const unsigned cb = obuf_addr(c);
        bf16x8 af0[6], af1[6];
        oload(cb, 0, af0);
        SB();
        f32x4 acc[2][3];
#pragma unroll
        for (int nb = 0; nb < 2; ++nb)
#pragma unroll
          for (int q = 0; q < 3; ++q) {
            const unsigned u0 = PP[nb][q][0][c], u1 = PP[nb][q][1][c];
            acc[nb][q] = f32x4{__uint_as_float(u0 << 16), __uint_as_float(u0 & 0xffff0000u), __uint_as_float(u1 << 16), __uint_as_float(u1 & 0xffff0000u)};
          }
#pragma unroll
        for (int ks = 0; ks < 6; ++ks) {
          SB();
          { const unsigned ad = oa[1] + cb + 4096u * ks; af1[ks] = cat(ldtr(ad), ldtr(ad + 2048u)); }
          SB();
#pragma unroll
          for (int q = 0; q < 3; ++q) acc[0][q] = mfma(af0[ks], Of[ks][q], acc[0][q]);
        }
        SB();
        omma(af1, acc[1]);
        SB();
#pragma unroll
        for (int q = 0; q < 3; ++q)
          __builtin_amdgcn_raw_buffer_store_b128(__builtin_bit_cast(u32x4, pack8(acc[0][q], acc[1][q])), srdP, l16,
                                                 pbase + (unsigned)(c * 3 + q) * 1024u, 16  );
      }
      waitvm<0>();
    }
    bar();
    const unsigned tag = E + 1u;
    if (hw == 0) {
      const unsigned l5 = lane_id();
      if (l5 == 0) __hip_atomic_store(epw + 32 * (1 + jj), tag, __ATOMIC_RELAXED, __HIP_MEMORY_SCOPE_AGENT);
      for (unsigned spin = 0; spin < (1u << 22); ++spin) {
        const unsigned v = __hip_atomic_load(epw + 32 * (1 + (l5 & 15)), __ATOMIC_RELAXED, __HIP_MEMORY_SCOPE_AGENT);
        if (__all(v == tag)) break;
        __builtin_amdgcn_s_sleep(1);
      }
      if (jj == 0 && l5 == 0) __hip_atomic_store(epw, tag, __ATOMIC_RELAXED, __HIP_MEMORY_SCOPE_AGENT);
      __builtin_amdgcn_fence(__ATOMIC_ACQUIRE, "agent");
      waitvm<0>();
    }
  }
  if (w < 4) bar();
  bar();
  STAMP(126);
  {
    const int piece = jj * 8 + w;
    const unsigned l6 = lane_id(), r6 = l6 & 15u, g6 = l6 >> 4;
    if (piece < 120) {
      float s[8];
#pragma unroll
      for (int i = 0; i < 8; ++i) s[i] = 0.f;
      u32x4 v[16];
#pragma unroll
      for (int k = 0; k < 16; ++k) {
        const unsigned obid = (unsigned)(((b * 2 + (k & 1)) << 3) | (k >> 1));
        v[k] = bload(srdP, l6 * 16u, obid * PART_BYTES + (unsigned)piece * 1024u);
      }
#pragma unroll
      for (int k = 0; k < 16; ++k)
#pragma unroll
        for (int i = 0; i < 4; ++i) { s[2 * i] += __uint_as_float(v[k][i] << 16); s[2 * i + 1] += __uint_as_float(v[k][i] & 0xffff0000u); }
      const int hp = piece / 30, rem = piece % 30, c = rem / 3, q = rem % 3;
      const int row = 48 * (hp & 1) + 16 * q + (int)r6, n0 = 64 * c + 32 * (hp >> 1) + 4 * (int)g6;
      __bf16* op = (__bf16*)outg + ((size_t)b * SQ + row) * DM + n0;
      *(bf16x4*)op = __builtin_convertvector((f32x4{s[0], s[1], s[2], s[3]}), bf16x4);
      *(bf16x4*)(op + 16) = __builtin_convertvector((f32x4{s[4], s[5], s[6], s[7]}), bf16x4);
    }
  }
  STAMP(127);
  STAMP_DUMP();
}

extern "C" void kernel_launch(void* const* d_in, const int* in_sizes, int n_in, void* d_out, int out_size, void* d_ws, size_t ws_size,
                              hipStream_t stream) {
  (void)in_sizes; (void)n_in; (void)out_size; (void)ws_size;
  (void)hipFuncSetAttribute((const void*)mha_kernel, hipFuncAttributeMaxDynamicSharedMemorySize, LDS_BYTES);
  mha_kernel<<<256, 512, LDS_BYTES, stream>>>(d_in[0], d_in[1], d_in[2], d_in[3], d_in[4], d_out, d_ws);
}
```

```cpp
#include <hip/hip_runtime.h>
#include <hip/hip_bf16.h>
#include <stdint.h>

typedef __bf16 bf16x8 __attribute__((ext_vector_type(8)));
typedef __bf16 bf16x4 __attribute__((ext_vector_type(4)));
typedef float f32x4 __attribute__((ext_vector_type(4)));
typedef float f32x2 __attribute__((ext_vector_type(2)));
typedef unsigned u32x4 __attribute__((ext_vector_type(4)));
typedef unsigned u32x2 __attribute__((ext_vector_type(2)));
typedef unsigned u32x10 __attribute__((ext_vector_type(10)));
#define AS3 __attribute__((address_space(3)))
#define AS1 __attribute__((address_space(1)))
#define DI __device__ __forceinline__
#define SB() __builtin_amdgcn_sched_barrier(0)

constexpr int NB = 16, SQ = 96, DM = 640, NH = 32, SK = 1024, DH = 192;
constexpr unsigned SLOT = 12288u, UNIT = 24576u;
constexpr unsigned WAREA = 3u * UNIT;
constexpr unsigned QBUF = 5u * UNIT;
constexpr unsigned MISC = QBUF + 36864u;
constexpr unsigned LDS_BYTES = 163840u;
constexpr unsigned KV_BYTES = (unsigned)NB * NH * SK * DH * 2u;
constexpr unsigned W_BYTES = (unsigned)DM * NH * DH * 2u;
constexpr unsigned X_BYTES = (unsigned)NB * SQ * DM * 2u;
constexpr unsigned PART_BYTES = 122880u;
constexpr unsigned WS_FLAGS = 0u;
constexpr unsigned WS_PART = 65536u;
constexpr float SM_C = 0.07216878364870322f * 1.4426950408889634f;
constexpr float RESC_THR = 6.0f / SM_C;

DI void dma16(unsigned ldsaddr, unsigned voff, __amdgpu_buffer_rsrc_t srd, unsigned soff) {
  asm volatile("s_mov_b32 m0, %0\n\ts_nop 0\n\tbuffer_load_dwordx4 %1, %2, %3 offen lds" :: "s"(ldsaddr), "v"(voff), "s"(srd), "s"(soff) : "memory", "m0");
}
DI void dma16nt(unsigned ldsaddr, unsigned voff, __amdgpu_buffer_rsrc_t srd, unsigned soff) {
  asm volatile("s_mov_b32 m0, %0\n\ts_nop 0\n\tbuffer_load_dwordx4 %1, %2, %3 offen nt lds" :: "s"(ldsaddr), "v"(voff), "s"(srd), "s"(soff) : "memory", "m0");
}
template <int N> DI void waitvm() { asm volatile("s_waitcnt vmcnt(%0)" :: "i"(N) : "memory"); }
DI void wait_vm_dyn(int n) {
  if (n >= 19) waitvm<19>(); else if (n >= 14) waitvm<14>(); else if (n >= 12) waitvm<12>(); else if (n == 11) waitvm<11>(); else if (n >= 6) waitvm<6>(); else if (n == 5) waitvm<5>(); else waitvm<0>();
}
DI void bar() { asm volatile("s_waitcnt lgkmcnt(0)\n\ts_barrier" ::: "memory"); }
DI unsigned lane_id() { unsigned l; asm volatile("v_mbcnt_lo_u32_b32 %0, -1, 0\n\tv_mbcnt_hi_u32_b32 %0, -1, %0" : "=v"(l)); return l; }
DI unsigned rfl(unsigned x) { return __builtin_amdgcn_readfirstlane(x); }
DI bf16x8 ld128(unsigned a) { return *(const AS3 bf16x8*)(size_t)a; }
DI f32x4 ldf4(unsigned a) { return *(const AS3 f32x4*)(size_t)a; }
DI void st128(unsigned a, bf16x8 v) { *(AS3 bf16x8*)(size_t)a = v; }
DI void stu4(unsigned a, u32x4 v) { *(AS3 u32x4*)(size_t)a = v; }
DI void stf4(unsigned a, f32x4 v) { *(AS3 f32x4*)(size_t)a = v; }
DI bf16x4 ldtr(unsigned a) { return __builtin_amdgcn_ds_read_tr16_b64_v4bf16((AS3 bf16x4*)(size_t)a); }
DI bf16x8 cat(bf16x4 lo, bf16x4 hi) { return __builtin_shufflevector(lo, hi, 0, 1, 2, 3, 4, 5, 6, 7); }
DI f32x4 mfma(bf16x8 a, bf16x8 b, f32x4 c) { return __builtin_amdgcn_mfma_f32_16x16x32_bf16(a, b, c, 0, 0, 0); }
DI u32x4 bload(__amdgpu_buffer_rsrc_t srd, unsigned voff, unsigned soff) {
  return __builtin_amdgcn_raw_buffer_load_b128(srd, voff, soff, 0);
}
DI bf16x8 pack8(f32x4 a, f32x4 b) {
  bf16x4 lo = __builtin_convertvector(a, bf16x4), hi = __builtin_convertvector(b, bf16x4);
  return cat(lo, hi);
}
DI f32x4 unpack4(bf16x4 v) {
  const u32x2 u = __builtin_bit_cast(u32x2, v);
  return f32x4{__uint_as_float(u[0] << 16), __uint_as_float(u[0] & 0xffff0000u), __uint_as_float(u[1] << 16), __uint_as_float(u[1] & 0xffff0000u)};
}
DI float max3f(float a, float b, float c) { float r; asm("v_max3_f32 %0, %1, %2, %3" : "=v"(r) : "v"(a), "v"(b), "v"(c)); return r; }
DI float max2f(float a, float b) { float r; asm("v_max_f32_e32 %0, %1, %2" : "=v"(r) : "v"(a), "v"(b)); return r; }
DI float xmax_g(float v) {
  auto a = __builtin_amdgcn_permlane16_swap(__float_as_uint(v), __float_as_uint(v), false, false);
  v = fmaxf(__uint_as_float(a[0]), __uint_as_float(a[1]));
  auto b = __builtin_amdgcn_permlane32_swap(__float_as_uint(v), __float_as_uint(v), false, false);
  return fmaxf(__uint_as_float(b[0]), __uint_as_float(b[1]));
}
DI float xsum_g(float v) {
  auto a = __builtin_amdgcn_permlane16_swap(__float_as_uint(v), __float_as_uint(v), false, false);
  v = __uint_as_float(a[0]) + __uint_as_float(a[1]);
  auto b = __builtin_amdgcn_permlane32_swap(__float_as_uint(v), __float_as_uint(v), false, false);
  return __uint_as_float(b[0]) + __uint_as_float(b[1]);
}
#define STAMP(i) do {} while (0)
#define STAMP_DUMP() do {} while (0)

__global__ __launch_bounds__(512, 2) void mha_kernel(const void* __restrict__ xg, const void* __restrict__ kg, const void* __restrict__ vg,
                                                     const void* __restrict__ wqg, const void* __restrict__ wog, void* __restrict__ outg,
                                                     void* __restrict__ ws) {
  extern __shared__ __attribute__((aligned(16))) char lds_raw[];
  const unsigned ldsb = (unsigned)(size_t)(AS3 char*)lds_raw;
  const int tid = threadIdx.x, lane = tid & 63;
  const int w = __builtin_amdgcn_readfirstlane(tid >> 6);
  const int r = lane & 15, g = lane >> 4;
  const int bid = blockIdx.x, xcd = bid & 7, idx = bid >> 3;
  const int b = idx >> 1, jj = 2 * xcd + (idx & 1), h0 = 2 * jj;
  const unsigned kvbase = (unsigned)(b * NH + h0) * (unsigned)(SK * DH * 2);
  const int lw = w & 3;
  STAMP(0);
  AS1 unsigned* const epw = (AS1 unsigned*)((char*)ws + WS_FLAGS) + (size_t)b * 17 * 32;
  const __amdgpu_buffer_rsrc_t srdP = __builtin_amdgcn_make_buffer_rsrc((char*)ws + WS_PART, 0, 256u * PART_BYTES, 0x00020000);
  const __amdgpu_buffer_rsrc_t srdW = __builtin_amdgcn_make_buffer_rsrc((void*)wqg, 0, W_BYTES, 0x00020000);
  const __amdgpu_buffer_rsrc_t srdX = __builtin_amdgcn_make_buffer_rsrc((void*)xg, 0, X_BYTES, 0x00020000);
  const __amdgpu_buffer_rsrc_t srdO = __builtin_amdgcn_make_buffer_rsrc((void*)wog, 0, W_BYTES, 0x00020000);
  const unsigned xbase = (unsigned)b * (unsigned)(SQ * DM * 2);
  auto obuf_addr = [&](int c) -> unsigned { const int k = c % 6; return k < 2 ? WAREA + (unsigned)k * UNIT : (k < 5 ? (unsigned)(k - 2) * UNIT : QBUF); };

  if (w < 4) {
    const int rh = w & 1, kh = w >> 1;
    {
      unsigned voW[3], voX[3];
#pragma unroll
      for (int i = 0; i < 3; ++i) {
        const unsigned p = 64u * (3u * lw + i) + lane;
        const unsigned row = p / 24u, c = p % 24u;
        voW[i] = row * 12288u + ((c ^ ((((row >> 1) & 1u) | (((row >> 3) & 1u) << 1)) << 1)) << 4);
        const unsigned rx = p >> 3, cx = p & 7u;
        voX[i] = rx * 1280u + ((cx ^ ((rx >> 1) & 7u)) << 4);
      }
      const unsigned stw = ldsb + 3072u * lw + lane * 16u;
      const unsigned wq0 = (unsigned)h0 * 384u;
      constexpr int QD = 3;
      u32x4 R[QD][9];
      auto ldc = [&](int s, int c) {
        const unsigned wo = (unsigned)(64 * c) * 12288u + wq0;
#pragma unroll
        for (int i = 0; i < 3; ++i) R[s][i] = bload(srdW, voW[i], wo);
#pragma unroll
        for (int i = 0; i < 3; ++i) R[s][3 + i] = bload(srdW, voW[i], wo + 32u * 12288u);
#pragma unroll
        for (int i = 0; i < 3; ++i) R[s][6 + i] = bload(srdX, voX[i], xbase + (unsigned)c * 128u);
      };
      auto stc = [&](int s, int c) {
        const unsigned base = stw + ((c & 1) ? WAREA : QBUF);
#pragma unroll
        for (int j = 0; j < 3; ++j)
#pragma unroll
          for (int i = 0; i < 3; ++i) stu4(base + (unsigned)j * SLOT + 1024u * i, R[s][3 * j + i]);
      };
      ldc(0, 0); ldc(1, 1);
      u32x4 wd[8], wx[15], we[8];
#pragma unroll
      for (int i = 0; i < 8; ++i) {
        const unsigned e = 60u * i + (lane < 60 ? lane : 59);
        const unsigned row = (unsigned)(40 * b + 10 * lw) + e / 48u;
        wd[i] = bload(srdW, row * 12288u + (e % 48u) * 16u, (unsigned)h0 * 384u);
      }
      {
        const unsigned so = (unsigned)idx * 61440u + (unsigned)lw * 15360u;
#pragma unroll
        for (int i = 0; i < 15; ++i) wx[i] = bload(srdX, lane * 16u, so + 1024u * i);
      }
#pragma unroll
      for (int c = 2; c < QD; ++c) ldc(c, c);
      {
        const unsigned so = (unsigned)(h0 * 192 + 24 * b) * 1280u + (unsigned)lw * 7680u;
#pragma unroll
        for (int i = 0; i < 8; ++i) we[i] = bload(srdO, (i < 7 || lane < 32 ? lane : 31) * 16u, so + 1024u * i);
      }
      SB();
      STAMP(104);
      stc(0, 0);
      STAMP(105);
      ldc(0, QD);
      bar();
      STAMP(106);
#pragma unroll
      for (int c = 0; c < 9; ++c) {
        stc((c + 1) % QD, c + 1);
        if (c + 1 + QD <= 9) ldc((c + 1) % QD, c + 1 + QD);
        if (c == 0) {
#pragma unroll
          for (int i = 0; i < 8; ++i) asm volatile("" :: "v"(wd[i]));
#pragma unroll
          for (int i = 0; i < 15; ++i) asm volatile("" :: "v"(wx[i]));
        }
        if (c == 3) {
#pragma unroll
          for (int i = 0; i < 8; ++i) asm volatile("" :: "v"(we[i]));
        }
        STAMP(107 + c);
        bar();
      }
    }
    STAMP(1);
    const unsigned ln = lane_id();
    const unsigned r2 = ln & 15u, g2 = ln >> 4;
    const unsigned fk = (r2 >> 1) & 7;
    const unsigned ka0 = ldsb + kh * SLOT + 384u * r2 + ((g2 ^ fk) << 4);
    const unsigned ka1 = ldsb + kh * SLOT + 384u * r2 + (((4u + g2) ^ fk) << 4);
    const unsigned qa = ldsb + QBUF + (unsigned)rh * 18432u + ln * 16u;
    const unsigned qq = r2 >> 2, pp = r2 & 3;
    const unsigned fv = (2u * (g2 & 1) + (qq >> 1)) << 1;
    unsigned va[4];
#pragma unroll
    for (int d = 0; d < 4; ++d) va[d] = ldsb + kh * SLOT + 384u * (4u * g2 + qq) + (((2u * d + (pp >> 1)) ^ fv) << 4) + 8u * (pp & 1);

#pragma unroll
    for (int pr = 0; pr < 2; ++pr) {
      f32x4 O[12][3];
#pragma unroll
      for (int d = 0; d < 12; ++d)
#pragma unroll
        for (int q = 0; q < 3; ++q) O[d][q] = f32x4{0.f, 0.f, 0.f, 0.f};
      float mrun[3] = {-INFINITY, -INFINITY, -INFINITY};
      f32x2 lsum2[3] = {f32x2{0.f, 0.f}, f32x2{0.f, 0.f}, f32x2{0.f, 0.f}};
      unsigned kpos = (unsigned)((32 * pr) % 3) * UNIT;
      for (int t = 0; t < 16; ++t) {
        const unsigned vpos = kpos == 2u * UNIT ? 0u : kpos + UNIT;
        f32x4 S[2][3];
        {
          bf16x8 kf[3][2], qf[3][3];
          auto ldq = [&](int s) {
#pragma unroll
            for (int q = 0; q < 3; ++q) qf[s % 3][q] = ld128(qa + (unsigned)(q * 6 + s) * 1024u);
          };
          const unsigned k0 = ka0 + kpos, k1 = ka1 + kpos;
          auto ldk = [&](int s) {
            const unsigned kb = ((s & 1) ? k1 : k0) + 128u * (s >> 1);
            kf[s % 3][0] = ld128(kb); kf[s % 3][1] = ld128(kb + 6144u);
          };
          if (t > 0) { ldq(0); ldq(1); }
          asm volatile("s_barrier" ::: "memory");
          STAMP(2 + 3 * (pr * 16 + t));
          if (t == 0) { ldq(0); ldq(1); }
          ldk(0); ldk(1);
#pragma unroll
          for (int q = 0; q < 3; ++q) { S[0][q] = f32x4{0.f, 0.f, 0.f, 0.f}; S[1][q] = f32x4{0.f, 0.f, 0.f, 0.f}; }
#pragma unroll
          for (int s = 0; s < 6; ++s) {
            SB();
            if (s + 2 < 6) { ldk(s + 2); ldq(s + 2); }
            SB();
#pragma unroll
            for (int q = 0; q < 3; ++q) {
              S[0][q] = mfma(kf[s % 3][0], qf[s % 3][q], S[0][q]);
              S[1][q] = mfma(kf[s % 3][1], qf[s % 3][q], S[1][q]);
            }
          }
          SB();
        }
        bar();
        STAMP(4 + 3 * (pr * 16 + t));
        bf16x8 vf[7];
        auto ldv = [&](int d) {
          const unsigned a = va[d & 3] + vpos + 128u * (d >> 2);
          vf[d % 7] = cat(ldtr(a), ldtr(a + 6144u));
        };
        ldv(0); ldv(1); ldv(2); ldv(3); ldv(4); ldv(5);
        SB();
        float mx[3];
        bool need = false;
#pragma unroll
        for (int q = 0; q < 3; ++q) {
          mx[q] = max3f(max3f(S[0][q][0], S[0][q][1], S[0][q][2]), max3f(S[0][q][3], S[1][q][0], S[1][q][1]), max2f(S[1][q][2], S[1][q][3]));
          need |= (mx[q] > mrun[q] + RESC_THR);
        }
        if (__any(need)) {
#pragma unroll
          for (int q = 0; q < 3; ++q) {
            const float mn = fmaxf(mrun[q], xmax_g(mx[q]));
            const float al = __builtin_amdgcn_exp2f((mrun[q] - mn) * SM_C);
            mrun[q] = mn;
            lsum2[q] *= al;
#pragma unroll
            for (int d = 0; d < 12; ++d) O[d][q] *= al;
          }
        }
        bf16x8 pf[3];
#pragma unroll
        for (int q = 0; q < 3; ++q) {
          const float nm = -mrun[q] * SM_C;
          const f32x2 nm2 = {nm, nm}, c2 = {SM_C, SM_C};
          f32x2 e[4];
#pragma unroll
          for (int i = 0; i < 4; ++i) {
            const f32x2 sv = {S[i >> 1][q][2 * (i & 1)], S[i >> 1][q][2 * (i & 1) + 1]};
            const f32x2 a = sv * c2 + nm2;
            e[i] = f32x2{__builtin_amdgcn_exp2f(a[0]), __builtin_amdgcn_exp2f(a[1])};
          }
          lsum2[q] += (e[0] + e[1]) + (e[2] + e[3]);
          pf[q] = pack8(f32x4{e[0][0], e[0][1], e[1][0], e[1][1]}, f32x4{e[2][0], e[2][1], e[3][0], e[3][1]});
        }
        STAMP(3 + 3 * (pr * 16 + t));
#pragma unroll
        for (int d = 0; d < 12; ++d) {
          SB();
          if (d + 6 < 12) ldv(d + 6);
          SB();
#pragma unroll
          for (int q = 0; q < 3; ++q) O[d][q] = mfma(vf[d % 7], pf[q], O[d][q]);
        }
        SB();
        asm volatile("s_waitcnt lgkmcnt(0)" ::: "memory");
        kpos = vpos == 2u * UNIT ? 0u : vpos + UNIT;
      }
      float lsum[3];
      {
        const unsigned sa = ldsb + MISC + (unsigned)w * 384u + r2 * 8u;
#pragma unroll
        for (int q = 0; q < 3; ++q) {
          lsum[q] = xsum_g(lsum2[q][0] + lsum2[q][1]);
          if (g2 == 0) *(AS3 f32x2*)(size_t)(sa + q * 128u) = f32x2{mrun[q], lsum[q]};
        }
      }
      bar();
      float sc[3];
      {
        const unsigned sb = ldsb + MISC + (unsigned)(w ^ 2) * 384u + r2 * 8u;
#pragma unroll
        for (int q = 0; q < 3; ++q) {
          const f32x2 o = *(const AS3 f32x2*)(size_t)(sb + q * 128u);
          const float mn = fmaxf(mrun[q], o[0]);
          const float a0 = __builtin_amdgcn_exp2f((mrun[q] - mn) * SM_C), a1 = __builtin_amdgcn_exp2f((o[0] - mn) * SM_C);
          const float l = lsum[q] * a0 + o[1] * a1;
          sc[q] = a0 / l;
        }
      }
      const unsigned xreg = pr == 0 ? WAREA : 0u;
      const unsigned xw = ldsb + xreg + (unsigned)w * 18432u + ln * 16u;
      const unsigned xr = ldsb + xreg + (unsigned)(w ^ 2) * 18432u + ln * 16u;
      if (kh) {
#pragma unroll
        for (int d = 0; d < 6; ++d)
#pragma unroll
          for (int q = 0; q < 3; ++q) { stf4(xw + (unsigned)(d * 3 + q) * 1024u, O[d][q] * sc[q]); O[d][q] = O[6 + d][q] * sc[q]; }
      } else {
#pragma unroll
        for (int d = 0; d < 6; ++d)
#pragma unroll
          for (int q = 0; q < 3; ++q) { stf4(xw + (unsigned)(d * 3 + q) * 1024u, O[6 + d][q] * sc[q]); O[d][q] = O[d][q] * sc[q]; }
      }
      bar();
#pragma unroll
      for (int d = 0; d < 6; ++d)
#pragma unroll
        for (int q = 0; q < 3; ++q) O[d][q] += ldf4(xr + (unsigned)(d * 3 + q) * 1024u);
      asm volatile("s_waitcnt lgkmcnt(0)" ::: "memory");
#pragma unroll
      for (int q = 0; q < 3; ++q)
#pragma unroll
        for (int s = 0; s < 3; ++s) st128(xr + (unsigned)(q * 3 + s) * 1024u, pack8(O[2 * s][q], O[2 * s + 1][q]));
      bar();
      STAMP(100 + pr);
      bar();
    }
    {
      const unsigned l4 = lane_id();
      const unsigned fo = ((l4 >> 4) & 3u) << 1;
      const unsigned voO = (l4 >> 3) * 1280u + (((l4 & 7u) ^ fo) << 4);
      const unsigned wob = (unsigned)(h0 + 1) * 192u * 1280u + (unsigned)lw * 48u * 1280u;
      auto issue = [&](int c) {
        const unsigned la = ldsb + obuf_addr(c) + (unsigned)lw * 6144u;
#pragma unroll
        for (int i = 0; i < 6; ++i) dma16(rfl(la + 1024u * i), voO, srdO, rfl(wob + (unsigned)i * 10240u + (unsigned)c * 128u));
      };
      issue(2); issue(3); issue(4); issue(5);
      for (int c = 0; c < 10; ++c) {
        if (c >= 1) {
          if (c <= 5) waitvm<18>(); else if (c == 6) waitvm<12>(); else if (c == 7) waitvm<6>(); else waitvm<0>();
        }
        bar();
        if (c >= 1 && c + 5 <= 9) issue(c + 5);
      }
    }
  } else {
    const int hw = w - 4, rh = hw & 1, xh = hw >> 1;
    __builtin_amdgcn_s_setprio(1);
    const __amdgpu_buffer_rsrc_t srdK = __builtin_amdgcn_make_buffer_rsrc((void*)kg, 0, KV_BYTES, 0x00020000);
    const __amdgpu_buffer_rsrc_t srdV = __builtin_amdgcn_make_buffer_rsrc((void*)vg, 0, KV_BYTES, 0x00020000);
    unsigned voK[3], voV[3];
#pragma unroll
    for (int i = 0; i < 3; ++i) {
      const unsigned p = 64u * i + lane;
      const unsigned row = p / 24u, c = p % 24u;
      voK[i] = (row + 16u * hw) * 384u + ((c ^ ((row >> 1) & 7u)) << 4);
      voV[i] = (row + 16u * hw) * 384u + ((c ^ (((row >> 1) & 3u) << 1)) << 4);
    }
    const unsigned ldsw = ldsb + 6144u * hw;
    auto issueKV = [&](int u) {
      const unsigned pos = ldsw + (unsigned)(u % 3) * UNIT;
      const unsigned go = kvbase + (unsigned)(u >> 1) * UNIT;
      if (u & 1) {
#pragma unroll
        for (int i = 0; i < 3; ++i) dma16nt(rfl(pos + 1024u * i), voV[i], srdV, rfl(go));
#pragma unroll
        for (int i = 0; i < 3; ++i) dma16nt(rfl(pos + 3072u + 1024u * i), voV[i], srdV, rfl(go + 3072u));
      } else {
#pragma unroll
        for (int i = 0; i < 3; ++i) dma16nt(rfl(pos + 1024u * i), voK[i], srdK, rfl(go));
#pragma unroll
        for (int i = 0; i < 3; ++i) dma16nt(rfl(pos + 3072u + 1024u * i), voK[i] ^ 64u, srdK, rfl(go + 3072u));
      }
    };
    issueKV(0); issueKV(1); issueKV(2);
    const unsigned qq = r >> 2, pp = r & 3;
    const unsigned fw = ((qq >> 1) | (((unsigned)g & 1u) << 1)) << 1;
    unsigned wa[6];
#pragma unroll
    for (int d = 0; d < 6; ++d) {
      const unsigned dg = 6u * xh + d;
      wa[d] = 384u * (8u * g + qq) + (((2u * (dg & 3u) + (pp >> 1)) ^ fw) << 4) + 8u * (pp & 1) + 128u * (dg >> 2);
    }
    unsigned E = rfl(__hip_atomic_load(epw, __ATOMIC_RELAXED, __HIP_MEMORY_SCOPE_AGENT));
    asm volatile("" : "+s"(E));
    f32x4 QA[6][3];
    auto zeroQA = [&]() {
#pragma unroll
      for (int d = 0; d < 6; ++d)
#pragma unroll
        for (int q = 0; q < 3; ++q) QA[d][q] = f32x4{0.f, 0.f, 0.f, 0.f};
    };
    auto writeQ = [&]() {
      const unsigned l = lane_id(), rr = l & 15u, gg = l >> 4;
      const unsigned qw = ldsb + QBUF + (unsigned)rh * 18432u + (rr + 16u * (gg >> 1)) * 16u + 8u * (gg & 1u) + (unsigned)xh * 3072u;
#pragma unroll
      for (int q = 0; q < 3; ++q)
#pragma unroll
        for (int d = 0; d < 6; ++d)
          *(AS3 bf16x4*)(size_t)(qw + (unsigned)(q * 6 + (d >> 1)) * 1024u + 512u * (d & 1)) = __builtin_convertvector(QA[d][q], bf16x4);
    };
    auto qload = [&](unsigned wb, bf16x8 (&af)[6]) {
#pragma unroll
      for (int i = 0; i < 6; ++i) { const unsigned ad = wb + wa[i]; af[i] = cat(ldtr(ad), ldtr(ad + 1536u)); }
    };
    auto qmma = [&](const bf16x8 (&af)[6], const bf16x8 (&xf)[3]) {
#pragma unroll
      for (int i = 0; i < 6; ++i)
#pragma unroll
        for (int q = 0; q < 3; ++q) QA[i][q] = mfma(af[i], xf[q], QA[i][q]);
    };
    zeroQA();
    {
      const unsigned fk = (r >> 1) & 7;
      const unsigned xa0 = 128u * (48u * rh + r) + (((unsigned)g ^ fk) << 4);
      const unsigned xa1 = 128u * (48u * rh + r) + (((4u + g) ^ fk) << 4);
      for (int c = 0; c < 10; ++c) {
        bar();
        STAMP(98 + c);
        const unsigned base = ldsb + ((c & 1) ? WAREA : QBUF);
        bf16x8 xf0[3], xf1[3], af0[6], af1[6];
#pragma unroll
        for (int q = 0; q < 3; ++q) xf0[q] = ld128(base + 2u * SLOT + xa0 + 2048u * q);
        qload(base, af0);
        SB();
#pragma unroll
        for (int q = 0; q < 3; ++q) xf1[q] = ld128(base + 2u * SLOT + xa1 + 2048u * q);
#pragma unroll
        for (int i = 0; i < 6; ++i) {
          SB();
          { const unsigned ad = base + SLOT + wa[i]; af1[i] = cat(ldtr(ad), ldtr(ad + 1536u)); }
          SB();
#pragma unroll
          for (int q = 0; q < 3; ++q) QA[i][q] = mfma(af0[i], xf0[q], QA[i][q]);
        }
        SB();
        qmma(af1, xf1);
        SB();
      }
    }
    writeQ();
    zeroQA();
    {
      unsigned voW[3], voX0, voX1;
#pragma unroll
      for (int i = 0; i < 3; ++i) {
        const unsigned p = 64u * (3u * hw + i) + lane;
        const unsigned row = p / 24u, c = p % 24u;
        voW[i] = row * 12288u + ((c ^ ((((row >> 1) & 1u) | (((row >> 3) & 1u) << 1)) << 1)) << 4);
      }
      {
        const unsigned p0 = 96u * hw + lane, p1 = p0 + 64u;
        voX0 = (p0 >> 2) * 1280u + (((p0 & 3u) ^ ((0u - (p0 >> 4)) & 3u)) << 4);
        voX1 = (p1 >> 2) * 1280u + (((p1 & 3u) ^ ((0u - (p1 >> 4)) & 3u)) << 4);
      }
      const unsigned xra = 64u * (48u * rh + r) + ((((unsigned)g) ^ ((0u - ((unsigned)r >> 2)) & 3u)) << 4);
      const unsigned wq1 = (unsigned)(h0 + 1) * 384u;
      int prev = 12;
      int jn = 0, jc = 0;
      for (int h = 0; h < 32; ++h) {
        wait_vm_dyn(prev);
        STAMP(1 + h);
        bar();
        int cnt = 0;
        const int hm = h % 3;
        const bool docomp = (h >= 2 && hm != 1 && jc < 20);
        bf16x8 xf[3], af[6];
        if (docomp) {
          const unsigned wbuf = ldsb + WAREA + (unsigned)(jc & 1) * 18432u;
#pragma unroll
          for (int q = 0; q < 3; ++q) xf[q] = ld128(wbuf + SLOT + xra + 1024u * q);
          qload(wbuf, af);
        }
        SB();
        if (hm != 2 && jn < 20) {
          const unsigned wbuf = ldsb + WAREA + (unsigned)(jn & 1) * 18432u;
          const unsigned so = (unsigned)(32 * jn) * 12288u + wq1;
#pragma unroll
          for (int i = 0; i < 3; ++i) dma16(rfl(wbuf + 3072u * hw + 1024u * i), voW[i], srdW, rfl(so));
          const unsigned xo = xbase + (unsigned)jn * 64u;
          dma16(rfl(wbuf + SLOT + 1536u * hw), voX0, srdX, rfl(xo));
          if (lane < 32) dma16(rfl(wbuf + SLOT + 1536u * hw + 1024u), voX1, srdX, rfl(xo));
          ++jn; cnt += 5;
        }
        if (h >= 1) { issueKV(h + 2); }
        cnt += 6;
        if (false && h == 26) {
          const unsigned so = (unsigned)(h0 * 192 + 24 * b) * 1280u + (unsigned)hw * 7680u;
#pragma unroll
          for (int i = 0; i < 7; ++i) dma16(rfl(ldsb + WAREA + 36864u + 3072u * hw), lane * 16u, srdO, rfl(so + 1024u * i));
          if (lane < 32) dma16(rfl(ldsb + WAREA + 36864u + 3072u * hw), lane * 16u, srdO, rfl(so + 7168u));
          cnt += 8;
        }
        SB();
        if (docomp) { qmma(af, xf); ++jc; }
        SB();
        prev = cnt;
      }
    }
    bar();
    issueKV(34);
    bar(); bar();
    bf16x8 Of[6][3];
    auto readO = [&](unsigned xreg) {
#pragma unroll
      for (int k2 = 0; k2 < 2; ++k2) {
        const unsigned src = ldsb + xreg + (unsigned)(rh + 2 * (1 - k2)) * 18432u + lane_id() * 16u;
#pragma unroll
        for (int q = 0; q < 3; ++q)
#pragma unroll
          for (int s = 0; s < 3; ++s) Of[3 * k2 + s][q] = ld128(src + (unsigned)(q * 3 + s) * 1024u);
      }
    };
    readO(WAREA);
    bar();
    writeQ();
    const unsigned l3 = lane_id(), g3 = l3 >> 4, qq3 = (l3 & 15u) >> 2, pp3 = l3 & 3u;
    const unsigned fv = (2u * (g3 & 1) + (qq3 >> 1)) << 1;
    unsigned oa[2];
#pragma unroll
    for (int nb = 0; nb < 2; ++nb) oa[nb] = ldsb + 128u * (4u * g3 + qq3) + (((4u * xh + 2u * nb + (pp3 >> 1)) ^ fv) << 4) + 8u * (pp3 & 1);
    const unsigned fo = ((l3 >> 4) & 3u) << 1;
    const unsigned voO = (l3 >> 3) * 1280u + (((l3 & 7u) ^ fo) << 4);
    auto oload = [&](unsigned cb, int nb, bf16x8 (&af)[6]) {
#pragma unroll
      for (int ks = 0; ks < 6; ++ks) { const unsigned ad = oa[nb] + cb + 4096u * ks; af[ks] = cat(ldtr(ad), ldtr(ad + 2048u)); }
    };
    auto omma = [&](const bf16x8 (&af)[6], f32x4 (&acc)[3]) {
#pragma unroll
      for (int ks = 0; ks < 6; ++ks)
#pragma unroll
        for (int q = 0; q < 3; ++q) acc[q] = mfma(af[ks], Of[ks][q], acc[q]);
    };
    u32x10 PP[2][3][2];
#pragma unroll
    for (int nb = 0; nb < 2; ++nb)
#pragma unroll
      for (int q = 0; q < 3; ++q)
#pragma unroll
        for (int d = 0; d < 2; ++d)
#pragma unroll
          for (int c = 0; c < 10; ++c) PP[nb][q][d][c] = 0u;
    {
      const unsigned wob0 = (unsigned)h0 * 192u * 1280u + (unsigned)hw * 48u * 1280u;
      auto half = [&](int h, int nb, int cc, bool comp, int ci, int prev) -> int {
        wait_vm_dyn(prev);
        STAMP(33 + h);
        bar();
        bf16x8 af[4];
        const unsigned oab = oa[nb] + WAREA + (unsigned)(cc & 1) * UNIT;
        auto ldo = [&](int ks) { af[ks & 3] = cat(ldtr(oab + 4096u * ks), ldtr(oab + 4096u * ks + 2048u)); };
        if (comp) { ldo(0); ldo(1); ldo(2); ldo(3); }
        SB();
        int cnt = 0;
        if (ci >= 0) {
          const unsigned wob = wob0 + (ci >= 10 ? 192u * 1280u + (unsigned)(ci - 10) * 128u : (unsigned)ci * 128u);
#pragma unroll
          for (int i = 0; i < 6; ++i) dma16(rfl(ldsb + WAREA + (unsigned)(ci & 1) * UNIT + 6144u * hw + 1024u * i), voO, srdO, rfl(wob + (unsigned)i * 10240u));
          cnt += 6;
        }
        if (h >= 1 && 34 + h < 64) issueKV(34 + h);
        if (34 + h < 64) cnt += 6;
        SB();
        if (comp) {
          f32x4 acc[3] = {f32x4{0.f, 0.f, 0.f, 0.f}, f32x4{0.f, 0.f, 0.f, 0.f}, f32x4{0.f, 0.f, 0.f, 0.f}};
#pragma unroll
          for (int ks = 0; ks < 6; ++ks) {
#pragma unroll
            for (int q = 0; q < 3; ++q) acc[q] = mfma(af[ks & 3], Of[ks][q], acc[q]);
            SB();
            if (ks + 4 < 6) ldo(ks + 4);
            SB();
          }
#pragma unroll
          for (int q = 0; q < 3; ++q) {
            const u32x2 pk = __builtin_bit_cast(u32x2, __builtin_convertvector(acc[q], bf16x4));
            PP[nb][q][0][cc] = pk[0]; PP[nb][q][1][cc] = pk[1];
          }
        }
        SB();
        STAMP(65 + h);
        return cnt;
      };
      int prev = 12;
      for (int i = 0; i < 16; ++i) {
        prev = half(2 * i, 1, i - 2, i >= 2 && i <= 11, -1, prev);
        prev = half(2 * i + 1, 0, i - 1, i >= 1 && i <= 10, i < 12 ? i : -1, prev);
      }
    }
    waitvm<0>();
    bar(); bar(); bar();
    readO(0u);
    bar();
    {
      const unsigned pbase = (unsigned)bid * PART_BYTES + (unsigned)hw * 30720u;
      const unsigned l16 = lane_id() * 16u;
      bf16x8 af[6];
      bar();
      auto ldstep = [&](unsigned cb, int st) {
        const unsigned ad = oa[st / 6] + cb + 4096u * (st % 6);
        af[st % 6] = cat(ldtr(ad), ldtr(ad + 2048u));
      };
#pragma unroll
      for (int st = 0; st < 5; ++st) ldstep(obuf_addr(0), st);
      for (int c = 0; c < 10; ++c) {
        STAMP(108 + c);
        const unsigned cb = obuf_addr(c), cn = obuf_addr(c + 1);
        bf16x4 pk[2][3];
#pragma unroll
        for (int nb = 0; nb < 2; ++nb) {
          f32x4 acc[3];
#pragma unroll
          for (int q = 0; q < 3; ++q) {
            const unsigned u0 = PP[nb][q][0][c], u1 = PP[nb][q][1][c];
            acc[q] = f32x4{__uint_as_float(u0 << 16), __uint_as_float(u0 & 0xffff0000u), __uint_as_float(u1 << 16), __uint_as_float(u1 & 0xffff0000u)};
          }
#pragma unroll
          for (int ks = 0; ks < 6; ++ks) {
            const int st = nb * 6 + ks;
            SB();
            if (st + 5 < 12) ldstep(cb, st + 5);
            else if (c < 9) ldstep(cn, st + 5 - 12);
            SB();
#pragma unroll
            for (int q = 0; q < 3; ++q) acc[q] = mfma(af[ks], Of[ks][q], acc[q]);
          }
          SB();
#pragma unroll
          for (int q = 0; q < 3; ++q) pk[nb][q] = __builtin_convertvector(acc[q], bf16x4);
        }
#pragma unroll
        for (int q = 0; q < 3; ++q)
          __builtin_amdgcn_raw_buffer_store_b128(__builtin_bit_cast(u32x4, cat(pk[0][q], pk[1][q])), srdP, l16,
                                                 pbase + (unsigned)(c * 3 + q) * 1024u, 16  );
        if (c < 9) bar();
      }
      waitvm<0>();
    }
    bar();
    const unsigned tag = E + 1u;
    if (hw == 0) {
      const unsigned l5 = lane_id();
      if (l5 == 0) __hip_atomic_store(epw + 32 * (1 + jj), tag, __ATOMIC_RELAXED, __HIP_MEMORY_SCOPE_AGENT);
      for (unsigned spin = 0; spin < (1u << 22); ++spin) {
        const unsigned v = __hip_atomic_load(epw + 32 * (1 + (l5 & 15)), __ATOMIC_RELAXED, __HIP_MEMORY_SCOPE_AGENT);
        if (__all(v == tag)) break;
        __builtin_amdgcn_s_sleep(1);
      }
      if (jj == 0 && l5 == 0) __hip_atomic_store(epw, tag, __ATOMIC_RELAXED, __HIP_MEMORY_SCOPE_AGENT);
      __builtin_amdgcn_fence(__ATOMIC_ACQUIRE, "wavefront");
    }
  }
  if (w < 4) bar();
  bar();
  STAMP(126);
  {
    const int piece = jj * 8 + w;
    const unsigned l6 = lane_id(), r6 = l6 & 15u, g6 = l6 >> 4;
    if (piece < 120) {
      float s[8];
#pragma unroll
      for (int i = 0; i < 8; ++i) s[i] = 0.f;
      u32x4 v[16];
#pragma unroll
      for (int k = 0; k < 16; ++k) {
        const unsigned obid = (unsigned)(((b * 2 + (k & 1)) << 3) | (k >> 1));
        v[k] = __builtin_amdgcn_raw_buffer_load_b128(srdP, l6 * 16u, obid * PART_BYTES + (unsigned)piece * 1024u, 16  );
      }
#pragma unroll
      for (int k = 0; k < 16; ++k)
#pragma unroll
        for (int i = 0; i < 4; ++i) { s[2 * i] += __uint_as_float(v[k][i] << 16); s[2 * i + 1] += __uint_as_float(v[k][i] & 0xffff0000u); }
      const int hp = piece / 30, rem = piece % 30, c = rem / 3, q = rem % 3;
      const int row = 48 * (hp & 1) + 16 * q + (int)r6, n0 = 64 * c + 32 * (hp >> 1) + 4 * (int)g6;
      __bf16* op = (__bf16*)outg + ((size_t)b * SQ + row) * DM + n0;
      *(bf16x4*)op = __builtin_convertvector((f32x4{s[0], s[1], s[2], s[3]}), bf16x4);
      *(bf16x4*)(op + 16) = __builtin_convertvector((f32x4{s[4], s[5], s[6], s[7]}), bf16x4);
    }
  }
  STAMP(127);
  STAMP_DUMP();
}

extern "C" void kernel_launch(void* const* d_in, const int* in_sizes, int n_in, void* d_out, int out_size, void* d_ws, size_t ws_size,
                              hipStream_t stream) {
  (void)in_sizes; (void)n_in; (void)out_size; (void)ws_size;
  (void)hipFuncSetAttribute((const void*)mha_kernel, hipFuncAttributeMaxDynamicSharedMemorySize, LDS_BYTES);
  mha_kernel<<<256, 512, LDS_BYTES, stream>>>(d_in[0], d_in[1], d_in[2], d_in[3], d_in[4], d_out, d_ws);
}
```

```cpp
#include <hip/hip_runtime.h>
#include <hip/hip_bf16.h>
#include <stdint.h>

typedef __bf16 bf16x8 __attribute__((ext_vector_type(8)));
typedef __bf16 bf16x4 __attribute__((ext_vector_type(4)));
typedef float f32x4 __attribute__((ext_vector_type(4)));
typedef float f32x2 __attribute__((ext_vector_type(2)));
typedef unsigned u32x4 __attribute__((ext_vector_type(4)));
typedef unsigned u32x2 __attribute__((ext_vector_type(2)));
typedef unsigned u32x10 __attribute__((ext_vector_type(10)));
#define AS3 __attribute__((address_space(3)))
#define AS1 __attribute__((address_space(1)))
#define DI __device__ __forceinline__
#define SB() __builtin_amdgcn_sched_barrier(0)

constexpr int NB = 16, SQ = 96, DM = 640, NH = 32, SK = 1024, DH = 192;
constexpr unsigned SLOT = 12288u, UNIT = 24576u;
constexpr unsigned WAREA = 3u * UNIT;
constexpr unsigned QBUF = 5u * UNIT;
constexpr unsigned MISC = QBUF + 36864u;
constexpr unsigned LDS_BYTES = 163840u;
constexpr unsigned KV_BYTES = (unsigned)NB * NH * SK * DH * 2u;
constexpr unsigned W_BYTES = (unsigned)DM * NH * DH * 2u;
constexpr unsigned X_BYTES = (unsigned)NB * SQ * DM * 2u;
constexpr unsigned PART_BYTES = 122880u;
constexpr unsigned WS_FLAGS = 0u;
constexpr unsigned WS_PART = 65536u;
constexpr float SM_C = 0.07216878364870322f * 1.4426950408889634f;
constexpr float RESC_THR = 6.0f / SM_C;

DI void dma16(unsigned ldsaddr, unsigned voff, __amdgpu_buffer_rsrc_t srd, unsigned soff) {
  asm volatile("s_mov_b32 m0, %0\n\ts_nop 0\n\tbuffer_load_dwordx4 %1, %2, %3 offen lds" :: "s"(ldsaddr), "v"(voff), "s"(srd), "s"(soff) : "memory", "m0");
}
DI void dma16nt(unsigned ldsaddr, unsigned voff, __amdgpu_buffer_rsrc_t srd, unsigned soff) {
  asm volatile("s_mov_b32 m0, %0\n\ts_nop 0\n\tbuffer_load_dwordx4 %1, %2, %3 offen nt lds" :: "s"(ldsaddr), "v"(voff), "s"(srd), "s"(soff) : "memory", "m0");
}
template <int N> DI void waitvm() { asm volatile("s_waitcnt vmcnt(%0)" :: "i"(N) : "memory"); }
DI void wait_vm_dyn(int n) {
  if (n >= 19) waitvm<19>(); else if (n >= 14) waitvm<14>(); else if (n >= 12) waitvm<12>(); else if (n == 11) waitvm<11>(); else if (n >= 6) waitvm<6>(); else if (n == 5) waitvm<5>(); else waitvm<0>();
}
DI void bar() { asm volatile("s_waitcnt lgkmcnt(0)\n\ts_barrier" ::: "memory"); }
DI unsigned lane_id() { unsigned l; asm volatile("v_mbcnt_lo_u32_b32 %0, -1, 0\n\tv_mbcnt_hi_u32_b32 %0, -1, %0" : "=v"(l)); return l; }
DI unsigned rfl(unsigned x) { return __builtin_amdgcn_readfirstlane(x); }
DI bf16x8 ld128(unsigned a) { return *(const AS3 bf16x8*)(size_t)a; }
DI f32x4 ldf4(unsigned a) { return *(const AS3 f32x4*)(size_t)a; }
DI void st128(unsigned a, bf16x8 v) { *(AS3 bf16x8*)(size_t)a = v; }
DI void stu4(unsigned a, u32x4 v) { *(AS3 u32x4*)(size_t)a = v; }
DI void stf4(unsigned a, f32x4 v) { *(AS3 f32x4*)(size_t)a = v; }
DI bf16x4 ldtr(unsigned a) { return __builtin_amdgcn_ds_read_tr16_b64_v4bf16((AS3 bf16x4*)(size_t)a); }
DI bf16x8 cat(bf16x4 lo, bf16x4 hi) { return __builtin_shufflevector(lo, hi, 0, 1, 2, 3, 4, 5, 6, 7); }
DI f32x4 mfma(bf16x8 a, bf16x8 b, f32x4 c) { return __builtin_amdgcn_mfma_f32_16x16x32_bf16(a, b, c, 0, 0, 0); }
DI u32x4 bload(__amdgpu_buffer_rsrc_t srd, unsigned voff, unsigned soff) {
  return __builtin_amdgcn_raw_buffer_load_b128(srd, voff, soff, 0);
}
DI bf16x8 pack8(f32x4 a, f32x4 b) {
  bf16x4 lo = __builtin_convertvector(a, bf16x4), hi = __builtin_convertvector(b, bf16x4);
  return cat(lo, hi);
}
DI f32x4 unpack4(bf16x4 v) {
  const u32x2 u = __builtin_bit_cast(u32x2, v);
  return f32x4{__uint_as_float(u[0] << 16), __uint_as_float(u[0] & 0xffff0000u), __uint_as_float(u[1] << 16), __uint_as_float(u[1] & 0xffff0000u)};
}
DI float max3f(float a, float b, float c) { float r; asm("v_max3_f32 %0, %1, %2, %3" : "=v"(r) : "v"(a), "v"(b), "v"(c)); return r; }
DI float max2f(float a, float b) { float r; asm("v_max_f32_e32 %0, %1, %2" : "=v"(r) : "v"(a), "v"(b)); return r; }
DI float xmax_g(float v) {
  auto a = __builtin_amdgcn_permlane16_swap(__float_as_uint(v), __float_as_uint(v), false, false);
  v = fmaxf(__uint_as_float(a[0]), __uint_as_float(a[1]));
  auto b = __builtin_amdgcn_permlane32_swap(__float_as_uint(v), __float_as_uint(v), false, false);
  return fmaxf(__uint_as_float(b[0]), __uint_as_float(b[1]));
}
DI float xsum_g(float v) {
  auto a = __builtin_amdgcn_permlane16_swap(__float_as_uint(v), __float_as_uint(v), false, false);
  v = __uint_as_float(a[0]) + __uint_as_float(a[1]);
  auto b = __builtin_amdgcn_permlane32_swap(__float_as_uint(v), __float_as_uint(v), false, false);
  return __uint_as_float(b[0]) + __uint_as_float(b[1]);
}
#define STAMP(i) do {} while (0)
#define STAMP_DUMP() do {} while (0)

__global__ __launch_bounds__(512, 2) void mha_kernel(const void* __restrict__ xg, const void* __restrict__ kg, const void* __restrict__ vg,
                                                     const void* __restrict__ wqg, const void* __restrict__ wog, void* __restrict__ outg,
                                                     void* __restrict__ ws) {
  extern __shared__ __attribute__((aligned(16))) char lds_raw[];
  const unsigned ldsb = (unsigned)(size_t)(AS3 char*)lds_raw;
  const int tid = threadIdx.x, lane = tid & 63;
  const int w = __builtin_amdgcn_readfirstlane(tid >> 6);
  const int r = lane & 15, g = lane >> 4;
  const int bid = blockIdx.x, xcd = bid & 7, idx = bid >> 3;
  const int b = idx >> 1, jj = 2 * xcd + (idx & 1), h0 = 2 * jj;
  const unsigned kvbase = (unsigned)(b * NH + h0) * (unsigned)(SK * DH * 2);
  const int lw = w & 3;
  STAMP(0);
  AS1 unsigned* const epw = (AS1 unsigned*)((char*)ws + WS_FLAGS) + (size_t)b * 17 * 32;
  const __amdgpu_buffer_rsrc_t srdP = __builtin_amdgcn_make_buffer_rsrc((char*)ws + WS_PART, 0, 256u * PART_BYTES, 0x00020000);
  const __amdgpu_buffer_rsrc_t srdW = __builtin_amdgcn_make_buffer_rsrc((void*)wqg, 0, W_BYTES, 0x00020000);
  const __amdgpu_buffer_rsrc_t srdX = __builtin_amdgcn_make_buffer_rsrc((void*)xg, 0, X_BYTES, 0x00020000);
  const __amdgpu_buffer_rsrc_t srdO = __builtin_amdgcn_make_buffer_rsrc((void*)wog, 0, W_BYTES, 0x00020000);
  const unsigned xbase = (unsigned)b * (unsigned)(SQ * DM * 2);
  auto obuf_addr = [&](int c) -> unsigned { const int k = c % 6; return k < 2 ? WAREA + (unsigned)k * UNIT : (k < 5 ? (unsigned)(k - 2) * UNIT : QBUF); };

  if (w < 4) {
    const int rh = w & 1, kh = w >> 1;
    {
      unsigned voW[3], voX[3];
#pragma unroll
      for (int i = 0; i < 3; ++i) {
        const unsigned p = 64u * (3u * lw + i) + lane;
        const unsigned row = p / 24u, c = p % 24u;
        voW[i] = row * 12288u + ((c ^ ((((row >> 1) & 1u) | (((row >> 3) & 1u) << 1)) << 1)) << 4);
        const unsigned rx = p >> 3, cx = p & 7u;
        voX[i] = rx * 1280u + ((cx ^ ((rx >> 1) & 7u)) << 4);
      }
      const unsigned stw = ldsb + 3072u * lw + lane * 16u;
      const unsigned wq0 = (unsigned)h0 * 384u;
      constexpr int QD = 5;
      u32x4 R[QD][9];
      auto ldc = [&](int s, int c) {
        const unsigned wo = (unsigned)(64 * c) * 12288u + wq0;
#pragma unroll
        for (int i = 0; i < 3; ++i) R[s][i] = bload(srdW, voW[i], wo);
#pragma unroll
        for (int i = 0; i < 3; ++i) R[s][3 + i] = bload(srdW, voW[i], wo + 32u * 12288u);
#pragma unroll
        for (int i = 0; i < 3; ++i) R[s][6 + i] = bload(srdX, voX[i], xbase + (unsigned)c * 128u);
      };
      auto stc = [&](int s, int c) {
        const unsigned base = stw + ((c & 1) ? WAREA : QBUF);
#pragma unroll
        for (int j = 0; j < 3; ++j)
#pragma unroll
          for (int i = 0; i < 3; ++i) stu4(base + (unsigned)j * SLOT + 1024u * i, R[s][3 * j + i]);
      };
#pragma unroll
      for (int c = 0; c < QD; ++c) ldc(c, c);
      u32x4 wd[4], we[8];
      SB();
      STAMP(104);
      stc(0, 0);
      STAMP(105);
      ldc(0, QD);
      bar();
      STAMP(106);
#pragma unroll
      for (int c = 0; c < 9; ++c) {
        stc((c + 1) % QD, c + 1);
        if (c + 1 + QD <= 9) ldc((c + 1) % QD, c + 1 + QD);
        if (c == 4) {
#pragma unroll
          for (int i = 0; i < 4; ++i) {
            const unsigned e = 60u * i + (lane < 60 ? lane : 59);
            const unsigned row = (unsigned)(40 * b + 10 * lw) + e / 24u;
            wd[i] = bload(srdW, row * 12288u + (e % 24u) * 16u, (unsigned)(h0 + 1) * 384u);
          }
          const unsigned so = (unsigned)(h0 * 192 + 24 * b) * 1280u + (unsigned)lw * 7680u;
#pragma unroll
          for (int i = 0; i < 8; ++i) we[i] = bload(srdO, (i < 7 || lane < 32 ? lane : 31) * 16u, so + 1024u * i);
        }
        STAMP(107 + c);
        bar();
      }
#pragma unroll
      for (int i = 0; i < 4; ++i) asm volatile("" :: "v"(wd[i]));
#pragma unroll
      for (int i = 0; i < 8; ++i) asm volatile("" :: "v"(we[i]));
    }
    STAMP(1);
    const unsigned ln = lane_id();
    const unsigned r2 = ln & 15u, g2 = ln >> 4;
    const unsigned fk = (r2 >> 1) & 7;
    const unsigned ka0 = ldsb + kh * SLOT + 384u * r2 + ((g2 ^ fk) << 4);
    const unsigned ka1 = ldsb + kh * SLOT + 384u * r2 + (((4u + g2) ^ fk) << 4);
    const unsigned qa = ldsb + QBUF + (unsigned)rh * 18432u + ln * 16u;
    const unsigned qq = r2 >> 2, pp = r2 & 3;
    const unsigned fv = (2u * (g2 & 1) + (qq >> 1)) << 1;
    unsigned va[4];
#pragma unroll
    for (int d = 0; d < 4; ++d) va[d] = ldsb + kh * SLOT + 384u * (4u * g2 + qq) + (((2u * d + (pp >> 1)) ^ fv) << 4) + 8u * (pp & 1);

#pragma unroll
    for (int pr = 0; pr < 2; ++pr) {
      f32x4 O[12][3];
#pragma unroll
      for (int d = 0; d < 12; ++d)
#pragma unroll
        for (int q = 0; q < 3; ++q) O[d][q] = f32x4{0.f, 0.f, 0.f, 0.f};
      float mrun[3] = {-INFINITY, -INFINITY, -INFINITY};
      f32x2 lsum2[3] = {f32x2{0.f, 0.f}, f32x2{0.f, 0.f}, f32x2{0.f, 0.f}};
      unsigned kpos = (unsigned)((32 * pr) % 3) * UNIT;
      for (int t = 0; t < 16; ++t) {
        const unsigned vpos = kpos == 2u * UNIT ? 0u : kpos + UNIT;
        f32x4 S[2][3];
        {
          bf16x8 kf[3][2], qf[3][3];
          auto ldq = [&](int s) {
#pragma unroll
            for (int q = 0; q < 3; ++q) qf[s % 3][q] = ld128(qa + (unsigned)(q * 6 + s) * 1024u);
          };
          const unsigned k0 = ka0 + kpos, k1 = ka1 + kpos;
          auto ldk = [&](int s) {
            const unsigned kb = ((s & 1) ? k1 : k0) + 128u * (s >> 1);
            kf[s % 3][0] = ld128(kb); kf[s % 3][1] = ld128(kb + 6144u);
          };
          if (t > 0) { ldq(0); ldq(1); }
          asm volatile("s_barrier" ::: "memory");
          STAMP(2 + 3 * (pr * 16 + t));
          if (t == 0) { ldq(0); ldq(1); }
          ldk(0); ldk(1);
#pragma unroll
          for (int q = 0; q < 3; ++q) { S[0][q] = f32x4{0.f, 0.f, 0.f, 0.f}; S[1][q] = f32x4{0.f, 0.f, 0.f, 0.f}; }
#pragma unroll
          for (int s = 0; s < 6; ++s) {
            SB();
            if (s + 2 < 6) { ldk(s + 2); ldq(s + 2); }
            SB();
#pragma unroll
            for (int q = 0; q < 3; ++q) {
              S[0][q] = mfma(kf[s % 3][0], qf[s % 3][q], S[0][q]);
              S[1][q] = mfma(kf[s % 3][1], qf[s % 3][q], S[1][q]);
            }
          }
          SB();
        }
        bar();
        STAMP(4 + 3 * (pr * 16 + t));
        bf16x8 vf[7];
        auto ldv = [&](int d) {
          const unsigned a = va[d & 3] + vpos + 128u * (d >> 2);
          vf[d % 7] = cat(ldtr(a), ldtr(a + 6144u));
        };
        ldv(0); ldv(1); ldv(2); ldv(3); ldv(4); ldv(5);
        SB();
        float mx[3];
        bool need = false;
#pragma unroll
        for (int q = 0; q < 3; ++q) {
          mx[q] = max3f(max3f(S[0][q][0], S[0][q][1], S[0][q][2]), max3f(S[0][q][3], S[1][q][0], S[1][q][1]), max2f(S[1][q][2], S[1][q][3]));
          need |= (mx[q] > mrun[q] + RESC_THR);
        }
        if (__any(need)) {
#pragma unroll
          for (int q = 0; q < 3; ++q) {
            const float mn = fmaxf(mrun[q], xmax_g(mx[q]));
            const float al = __builtin_amdgcn_exp2f((mrun[q] - mn) * SM_C);
            mrun[q] = mn;
            lsum2[q] *= al;
#pragma unroll
            for (int d = 0; d < 12; ++d) O[d][q] *= al;
          }
        }
        bf16x8 pf[3];
#pragma unroll
        for (int q = 0; q < 3; ++q) {
          const float nm = -mrun[q] * SM_C;
          const f32x2 nm2 = {nm, nm}, c2 = {SM_C, SM_C};
          f32x2 e[4];
#pragma unroll
          for (int i = 0; i < 4; ++i) {
            const f32x2 sv = {S[i >> 1][q][2 * (i & 1)], S[i >> 1][q][2 * (i & 1) + 1]};
            const f32x2 a = sv * c2 + nm2;
            e[i] = f32x2{__builtin_amdgcn_exp2f(a[0]), __builtin_amdgcn_exp2f(a[1])};
          }
          lsum2[q] += (e[0] + e[1]) + (e[2] + e[3]);
          pf[q] = pack8(f32x4{e[0][0], e[0][1], e[1][0], e[1][1]}, f32x4{e[2][0], e[2][1], e[3][0], e[3][1]});
        }
        STAMP(3 + 3 * (pr * 16 + t));
#pragma unroll
        for (int d = 0; d < 12; ++d) {
          SB();
          if (d + 6 < 12) ldv(d + 6);
          SB();
#pragma unroll
          for (int q = 0; q < 3; ++q) O[d][q] = mfma(vf[d % 7], pf[q], O[d][q]);
        }
        SB();
        asm volatile("s_waitcnt lgkmcnt(0)" ::: "memory");
        kpos = vpos == 2u * UNIT ? 0u : vpos + UNIT;
      }
      float lsum[3];
      {
        const unsigned sa = ldsb + MISC + (unsigned)w * 384u + r2 * 8u;
#pragma unroll
        for (int q = 0; q < 3; ++q) {
          lsum[q] = xsum_g(lsum2[q][0] + lsum2[q][1]);
          if (g2 == 0) *(AS3 f32x2*)(size_t)(sa + q * 128u) = f32x2{mrun[q], lsum[q]};
        }
      }
      bar();
      float sc[3];
      {
        const unsigned sb = ldsb + MISC + (unsigned)(w ^ 2) * 384u + r2 * 8u;
#pragma unroll
        for (int q = 0; q < 3; ++q) {
          const f32x2 o = *(const AS3 f32x2*)(size_t)(sb + q * 128u);
          const float mn = fmaxf(mrun[q], o[0]);
          const float a0 = __builtin_amdgcn_exp2f((mrun[q] - mn) * SM_C), a1 = __builtin_amdgcn_exp2f((o[0] - mn) * SM_C);
          const float l = lsum[q] * a0 + o[1] * a1;
          sc[q] = a0 / l;
        }
      }
      const unsigned xreg = pr == 0 ? WAREA : 0u;
      const unsigned xw = ldsb + xreg + (unsigned)w * 18432u + ln * 16u;
      const unsigned xr = ldsb + xreg + (unsigned)(w ^ 2) * 18432u + ln * 16u;
      if (kh) {
#pragma unroll
        for (int d = 0; d < 6; ++d)
#pragma unroll
          for (int q = 0; q < 3; ++q) { stf4(xw + (unsigned)(d * 3 + q) * 1024u, O[d][q] * sc[q]); O[d][q] = O[6 + d][q] * sc[q]; }
      } else {
#pragma unroll
        for (int d = 0; d < 6; ++d)
#pragma unroll
          for (int q = 0; q < 3; ++q) { stf4(xw + (unsigned)(d * 3 + q) * 1024u, O[6 + d][q] * sc[q]); O[d][q] = O[d][q] * sc[q]; }
      }
      bar();
#pragma unroll
      for (int d = 0; d < 6; ++d)
#pragma unroll
        for (int q = 0; q < 3; ++q) O[d][q] += ldf4(xr + (unsigned)(d * 3 + q) * 1024u);
      asm volatile("s_waitcnt lgkmcnt(0)" ::: "memory");
#pragma unroll
      for (int q = 0; q < 3; ++q)
#pragma unroll
        for (int s = 0; s < 3; ++s) st128(xr + (unsigned)(q * 3 + s) * 1024u, pack8(O[2 * s][q], O[2 * s + 1][q]));
      bar();
      STAMP(100 + pr);
      bar();
    }
    {
      const unsigned l4 = lane_id();
      const unsigned fo = ((l4 >> 4) & 3u) << 1;
      const unsigned voO = (l4 >> 3) * 1280u + (((l4 & 7u) ^ fo) << 4);
      const unsigned wob = (unsigned)(h0 + 1) * 192u * 1280u + (unsigned)lw * 48u * 1280u;
      auto issue = [&](int c) {
        const unsigned la = ldsb + obuf_addr(c) + (unsigned)lw * 6144u;
#pragma unroll
        for (int i = 0; i < 6; ++i) dma16(rfl(la + 1024u * i), voO, srdO, rfl(wob + (unsigned)i * 10240u + (unsigned)c * 128u));
      };
      issue(2); issue(3); issue(4); issue(5);
      for (int c = 0; c < 10; ++c) {
        if (c >= 1) {
          if (c <= 5) waitvm<18>(); else if (c == 6) waitvm<12>(); else if (c == 7) waitvm<6>(); else waitvm<0>();
        }
        bar();
        if (c >= 1 && c + 5 <= 9) issue(c + 5);
      }
    }
  } else {
    const int hw = w - 4, rh = hw & 1, xh = hw >> 1;
    __builtin_amdgcn_s_setprio(1);
    const __amdgpu_buffer_rsrc_t srdK = __builtin_amdgcn_make_buffer_rsrc((void*)kg, 0, KV_BYTES, 0x00020000);
    const __amdgpu_buffer_rsrc_t srdV = __builtin_amdgcn_make_buffer_rsrc((void*)vg, 0, KV_BYTES, 0x00020000);
    unsigned voK[3], voV[3];
#pragma unroll
    for (int i = 0; i < 3; ++i) {
      const unsigned p = 64u * i + lane;
      const unsigned row = p / 24u, c = p % 24u;
      voK[i] = (row + 16u * hw) * 384u + ((c ^ ((row >> 1) & 7u)) << 4);
      voV[i] = (row + 16u * hw) * 384u + ((c ^ (((row >> 1) & 3u) << 1)) << 4);
    }
    const unsigned ldsw = ldsb + 6144u * hw;
    auto issueKV = [&](int u) {
      const unsigned pos = ldsw + (unsigned)(u % 3) * UNIT;
      const unsigned go = kvbase + (unsigned)(u >> 1) * UNIT;
      if (u & 1) {
#pragma unroll
        for (int i = 0; i < 3; ++i) dma16nt(rfl(pos + 1024u * i), voV[i], srdV, rfl(go));
#pragma unroll
        for (int i = 0; i < 3; ++i) dma16nt(rfl(pos + 3072u + 1024u * i), voV[i], srdV, rfl(go + 3072u));
      } else {
#pragma unroll
        for (int i = 0; i < 3; ++i) dma16nt(rfl(pos + 1024u * i), voK[i], srdK, rfl(go));
#pragma unroll
        for (int i = 0; i < 3; ++i) dma16nt(rfl(pos + 3072u + 1024u * i), voK[i] ^ 64u, srdK, rfl(go + 3072u));
      }
    };
    issueKV(0); issueKV(1); issueKV(2);
    const unsigned qq = r >> 2, pp = r & 3;
    const unsigned fw = ((qq >> 1) | (((unsigned)g & 1u) << 1)) << 1;
    unsigned wa[6];
#pragma unroll
    for (int d = 0; d < 6; ++d) {
      const unsigned dg = 6u * xh + d;
      wa[d] = 384u * (8u * g + qq) + (((2u * (dg & 3u) + (pp >> 1)) ^ fw) << 4) + 8u * (pp & 1) + 128u * (dg >> 2);
    }
    unsigned E = rfl(__hip_atomic_load(epw, __ATOMIC_RELAXED, __HIP_MEMORY_SCOPE_AGENT));
    asm volatile("" : "+s"(E));
    f32x4 QA[6][3];
    auto zeroQA = [&]() {
#pragma unroll
      for (int d = 0; d < 6; ++d)
#pragma unroll
        for (int q = 0; q < 3; ++q) QA[d][q] = f32x4{0.f, 0.f, 0.f, 0.f};
    };
    auto writeQ = [&]() {
      const unsigned l = lane_id(), rr = l & 15u, gg = l >> 4;
      const unsigned qw = ldsb + QBUF + (unsigned)rh * 18432u + (rr + 16u * (gg >> 1)) * 16u + 8u * (gg & 1u) + (unsigned)xh * 3072u;
#pragma unroll
      for (int q = 0; q < 3; ++q)
#pragma unroll
        for (int d = 0; d < 6; ++d)
          *(AS3 bf16x4*)(size_t)(qw + (unsigned)(q * 6 + (d >> 1)) * 1024u + 512u * (d & 1)) = __builtin_convertvector(QA[d][q], bf16x4);
    };
    auto qload = [&](unsigned wb, bf16x8 (&af)[6]) {
#pragma unroll
      for (int i = 0; i < 6; ++i) { const unsigned ad = wb + wa[i]; af[i] = cat(ldtr(ad), ldtr(ad + 1536u)); }
    };
    auto qmma = [&](const bf16x8 (&af)[6], const bf16x8 (&xf)[3]) {
#pragma unroll
      for (int i = 0; i < 6; ++i)
#pragma unroll
        for (int q = 0; q < 3; ++q) QA[i][q] = mfma(af[i], xf[q], QA[i][q]);
    };
    zeroQA();
    {
      const unsigned fk = (r >> 1) & 7;
      const unsigned xa0 = 128u * (48u * rh + r) + (((unsigned)g ^ fk) << 4);
      const unsigned xa1 = 128u * (48u * rh + r) + (((4u + g) ^ fk) << 4);
      for (int c = 0; c < 10; ++c) {
        bar();
        STAMP(98 + c);
        const unsigned base = ldsb + ((c & 1) ? WAREA : QBUF);
        bf16x8 xf0[3], xf1[3], af0[6], af1[6];
#pragma unroll
        for (int q = 0; q < 3; ++q) xf0[q] = ld128(base + 2u * SLOT + xa0 + 2048u * q);
        qload(base, af0);
        SB();
#pragma unroll
        for (int q = 0; q < 3; ++q) xf1[q] = ld128(base + 2u * SLOT + xa1 + 2048u * q);
#pragma unroll
        for (int i = 0; i < 6; ++i) {
          SB();
          { const unsigned ad = base + SLOT + wa[i]; af1[i] = cat(ldtr(ad), ldtr(ad + 1536u)); }
          SB();
#pragma unroll
          for (int q = 0; q < 3; ++q) QA[i][q] = mfma(af0[i], xf0[q], QA[i][q]);
        }
        SB();
        qmma(af1, xf1);
        SB();
      }
    }
    writeQ();
    zeroQA();
    {
      unsigned voW[3], voX0, voX1;
#pragma unroll
      for (int i = 0; i < 3; ++i) {
        const unsigned p = 64u * (3u * hw + i) + lane;
        const unsigned row = p / 24u, c = p % 24u;
        voW[i] = row * 12288u + ((c ^ ((((row >> 1) & 1u) | (((row >> 3) & 1u) << 1)) << 1)) << 4);
      }
      {
        const unsigned p0 = 96u * hw + lane, p1 = p0 + 64u;
        voX0 = (p0 >> 2) * 1280u + (((p0 & 3u) ^ ((0u - (p0 >> 4)) & 3u)) << 4);
        voX1 = (p1 >> 2) * 1280u + (((p1 & 3u) ^ ((0u - (p1 >> 4)) & 3u)) << 4);
      }
      const unsigned xra = 64u * (48u * rh + r) + ((((unsigned)g) ^ ((0u - ((unsigned)r >> 2)) & 3u)) << 4);
      const unsigned wq1 = (unsigned)(h0 + 1) * 384u;
      int prev = 12;
      int jn = 0, jc = 0;
      for (int h = 0; h < 32; ++h) {
        wait_vm_dyn(prev);
        STAMP(1 + h);
        bar();
        int cnt = 0;
        const int hm = h % 3;
        const bool docomp = (h >= 2 && hm != 1 && jc < 20);
        bf16x8 xf[3], af[6];
        if (docomp) {
          const unsigned wbuf = ldsb + WAREA + (unsigned)(jc & 1) * 18432u;
#pragma unroll
          for (int q = 0; q < 3; ++q) xf[q] = ld128(wbuf + SLOT + xra + 1024u * q);
          qload(wbuf, af);
        }
        SB();
        if (hm != 2 && jn < 20) {
          const unsigned wbuf = ldsb + WAREA + (unsigned)(jn & 1) * 18432u;
          const unsigned so = (unsigned)(32 * jn) * 12288u + wq1;
#pragma unroll
          for (int i = 0; i < 3; ++i) dma16(rfl(wbuf + 3072u * hw + 1024u * i), voW[i], srdW, rfl(so));
          const unsigned xo = xbase + (unsigned)jn * 64u;
          dma16(rfl(wbuf + SLOT + 1536u * hw), voX0, srdX, rfl(xo));
          if (lane < 32) dma16(rfl(wbuf + SLOT + 1536u * hw + 1024u), voX1, srdX, rfl(xo));
          ++jn; cnt += 5;
        }
        if (h >= 1) { issueKV(h + 2); }
        cnt += 6;
        if (false && h == 26) {
          const unsigned so = (unsigned)(h0 * 192 + 24 * b) * 1280u + (unsigned)hw * 7680u;
#pragma unroll
          for (int i = 0; i < 7; ++i) dma16(rfl(ldsb + WAREA + 36864u + 3072u * hw), lane * 16u, srdO, rfl(so + 1024u * i));
          if (lane < 32) dma16(rfl(ldsb + WAREA + 36864u + 3072u * hw), lane * 16u, srdO, rfl(so + 7168u));
          cnt += 8;
        }
        SB();
        if (docomp) { qmma(af, xf); ++jc; }
        SB();
        prev = cnt;
      }
    }
    bar();
    issueKV(34);
    bar(); bar();
    bf16x8 Of[6][3];
    auto readO = [&](unsigned xreg) {
#pragma unroll
      for (int k2 = 0; k2 < 2; ++k2) {
        const unsigned src = ldsb + xreg + (unsigned)(rh + 2 * (1 - k2)) * 18432u + lane_id() * 16u;
#pragma unroll
        for (int q = 0; q < 3; ++q)
#pragma unroll
          for (int s = 0; s < 3; ++s) Of[3 * k2 + s][q] = ld128(src + (unsigned)(q * 3 + s) * 1024u);
      }
    };
    readO(WAREA);
    bar();
    writeQ();
    const unsigned l3 = lane_id(), g3 = l3 >> 4, qq3 = (l3 & 15u) >> 2, pp3 = l3 & 3u;
    const unsigned fv = (2u * (g3 & 1) + (qq3 >> 1)) << 1;
    unsigned oa[2];
#pragma unroll
    for (int nb = 0; nb < 2; ++nb) oa[nb] = ldsb + 128u * (4u * g3 + qq3) + (((4u * xh + 2u * nb + (pp3 >> 1)) ^ fv) << 4) + 8u * (pp3 & 1);
    const unsigned fo = ((l3 >> 4) & 3u) << 1;
    const unsigned voO = (l3 >> 3) * 1280u + (((l3 & 7u) ^ fo) << 4);
    auto oload = [&](unsigned cb, int nb, bf16x8 (&af)[6]) {
#pragma unroll
      for (int ks = 0; ks < 6; ++ks) { const unsigned ad = oa[nb] + cb + 4096u * ks; af[ks] = cat(ldtr(ad), ldtr(ad + 2048u)); }
    };
    auto omma = [&](const bf16x8 (&af)[6], f32x4 (&acc)[3]) {
#pragma unroll
      for (int ks = 0; ks < 6; ++ks)
#pragma unroll
        for (int q = 0; q < 3; ++q) acc[q] = mfma(af[ks], Of[ks][q], acc[q]);
    };
    u32x10 PP[2][3][2];
#pragma unroll
    for (int nb = 0; nb < 2; ++nb)
#pragma unroll
      for (int q = 0; q < 3; ++q)
#pragma unroll
        for (int d = 0; d < 2; ++d)
#pragma unroll
          for (int c = 0; c < 10; ++c) PP[nb][q][d][c] = 0u;
    {
      const unsigned wob0 = (unsigned)h0 * 192u * 1280u + (unsigned)hw * 48u * 1280u;
      auto half = [&](int h, int nb, int cc, bool comp, int ci, int prev) -> int {
        wait_vm_dyn(prev);
        STAMP(33 + h);
        bar();
        bf16x8 af[4];
        const unsigned oab = oa[nb] + WAREA + (unsigned)(cc & 1) * UNIT;
        auto ldo = [&](int ks) { af[ks & 3] = cat(ldtr(oab + 4096u * ks), ldtr(oab + 4096u * ks + 2048u)); };
        if (comp) { ldo(0); ldo(1); ldo(2); ldo(3); }
        SB();
        int cnt = 0;
        if (ci >= 0) {
          const unsigned wob = wob0 + (ci >= 10 ? 192u * 1280u + (unsigned)(ci - 10) * 128u : (unsigned)ci * 128u);
#pragma unroll
          for (int i = 0; i < 6; ++i) dma16(rfl(ldsb + WAREA + (unsigned)(ci & 1) * UNIT + 6144u * hw + 1024u * i), voO, srdO, rfl(wob + (unsigned)i * 10240u));
          cnt += 6;
        }
        if (h >= 1 && 34 + h < 64) issueKV(34 + h);
        if (34 + h < 64) cnt += 6;
        SB();
        if (comp) {
          f32x4 acc[3] = {f32x4{0.f, 0.f, 0.f, 0.f}, f32x4{0.f, 0.f, 0.f, 0.f}, f32x4{0.f, 0.f, 0.f, 0.f}};
#pragma unroll
          for (int ks = 0; ks < 6; ++ks) {
#pragma unroll
            for (int q = 0; q < 3; ++q) acc[q] = mfma(af[ks & 3], Of[ks][q], acc[q]);
            SB();
            if (ks + 4 < 6) ldo(ks + 4);
            SB();
          }
#pragma unroll
          for (int q = 0; q < 3; ++q) {
            const u32x2 pk = __builtin_bit_cast(u32x2, __builtin_convertvector(acc[q], bf16x4));
            PP[nb][q][0][cc] = pk[0]; PP[nb][q][1][cc] = pk[1];
          }
        }
        SB();
        STAMP(65 + h);
        return cnt;
      };
      int prev = 12;
      for (int i = 0; i < 16; ++i) {
        prev = half(2 * i, 1, i - 2, i >= 2 && i <= 11, -1, prev);
        prev = half(2 * i + 1, 0, i - 1, i >= 1 && i <= 10, i < 12 ? i : -1, prev);
      }
    }
    waitvm<0>();
    bar(); bar(); bar();
    readO(0u);
    bar();
    {
      const unsigned pbase = (unsigned)bid * PART_BYTES + (unsigned)hw * 30720u;
      const unsigned l16 = lane_id() * 16u;
      bf16x8 af[6];
      bar();
      auto ldstep = [&](unsigned cb, int st) {
        const unsigned ad = oa[st / 6] + cb + 4096u * (st % 6);
        af[st % 6] = cat(ldtr(ad), ldtr(ad + 2048u));
      };
#pragma unroll
      for (int st = 0; st < 5; ++st) ldstep(obuf_addr(0), st);
      for (int c = 0; c < 10; ++c) {
        STAMP(108 + c);
        const unsigned cb = obuf_addr(c), cn = obuf_addr(c + 1);
        bf16x4 pk[2][3];
#pragma unroll
        for (int nb = 0; nb < 2; ++nb) {
          f32x4 acc[3];
#pragma unroll
          for (int q = 0; q < 3; ++q) {
            const unsigned u0 = PP[nb][q][0][c], u1 = PP[nb][q][1][c];
            acc[q] = f32x4{__uint_as_float(u0 << 16), __uint_as_float(u0 & 0xffff0000u), __uint_as_float(u1 << 16), __uint_as_float(u1 & 0xffff0000u)};
          }
#pragma unroll
          for (int ks = 0; ks < 6; ++ks) {
            const int st = nb * 6 + ks;
            SB();
            if (st + 5 < 12) ldstep(cb, st + 5);
            else if (c < 9) ldstep(cn, st + 5 - 12);
            SB();
#pragma unroll
            for (int q = 0; q < 3; ++q) acc[q] = mfma(af[ks], Of[ks][q], acc[q]);
          }
          SB();
#pragma unroll
          for (int q = 0; q < 3; ++q) pk[nb][q] = __builtin_convertvector(acc[q], bf16x4);
        }
#pragma unroll
        for (int q = 0; q < 3; ++q)
          __builtin_amdgcn_raw_buffer_store_b128(__builtin_bit_cast(u32x4, cat(pk[0][q], pk[1][q])), srdP, l16,
                                                 pbase + (unsigned)(c * 3 + q) * 1024u, 16  );
        if (c < 9) bar();
      }
      waitvm<0>();
    }
    bar();
    const unsigned tag = E + 1u;
    if (hw == 0) {
      const unsigned l5 = lane_id();
      if (l5 == 0) __hip_atomic_store(epw + 32 * (1 + jj), tag, __ATOMIC_RELAXED, __HIP_MEMORY_SCOPE_AGENT);
      for (unsigned spin = 0; spin < (1u << 22); ++spin) {
        const unsigned v = __hip_atomic_load(epw + 32 * (1 + (l5 & 15)), __ATOMIC_RELAXED, __HIP_MEMORY_SCOPE_AGENT);
        if (__all(v == tag)) break;
        __builtin_amdgcn_s_sleep(1);
      }
      if (jj == 0 && l5 == 0) __hip_atomic_store(epw, tag, __ATOMIC_RELAXED, __HIP_MEMORY_SCOPE_AGENT);
      __builtin_amdgcn_fence(__ATOMIC_ACQUIRE, "wavefront");
    }
  }
  if (w < 4) bar();
  bar();
  STAMP(126);
  {
    const int piece = jj * 8 + w;
    const unsigned l6 = lane_id(), r6 = l6 & 15u, g6 = l6 >> 4;
    if (piece < 120) {
      float s[8];
#pragma unroll
      for (int i = 0; i < 8; ++i) s[i] = 0.f;
      u32x4 v[16];
#pragma unroll
      for (int k = 0; k < 16; ++k) {
        const unsigned obid = (unsigned)(((b * 2 + (k & 1)) << 3) | (k >> 1));
        v[k] = __builtin_amdgcn_raw_buffer_load_b128(srdP, l6 * 16u, obid * PART_BYTES + (unsigned)piece * 1024u, 16  );
      }
#pragma unroll
      for (int k = 0; k < 16; ++k)
#pragma unroll
        for (int i = 0; i < 4; ++i) { s[2 * i] += __uint_as_float(v[k][i] << 16); s[2 * i + 1] += __uint_as_float(v[k][i] & 0xffff0000u); }
      const int hp = piece / 30, rem = piece % 30, c = rem / 3, q = rem % 3;
      const int row = 48 * (hp & 1) + 16 * q + (int)r6, n0 = 64 * c + 32 * (hp >> 1) + 4 * (int)g6;
      __bf16* op = (__bf16*)outg + ((size_t)b * SQ + row) * DM + n0;
      *(bf16x4*)op = __builtin_convertvector((f32x4{s[0], s[1], s[2], s[3]}), bf16x4);
      *(bf16x4*)(op + 16) = __builtin_convertvector((f32x4{s[4], s[5], s[6], s[7]}), bf16x4);
    }
  }
  STAMP(127);
  STAMP_DUMP();
}

extern "C" void kernel_launch(void* const* d_in, const int* in_sizes, int n_in, void* d_out, int out_size, void* d_ws, size_t ws_size,
                              hipStream_t stream) {
  (void)in_sizes; (void)n_in; (void)out_size; (void)ws_size;
  (void)hipFuncSetAttribute((const void*)mha_kernel, hipFuncAttributeMaxDynamicSharedMemorySize, LDS_BYTES);
  mha_kernel<<<256, 512, LDS_BYTES, stream>>>(d_in[0], d_in[1], d_in[2], d_in[3], d_in[4], d_out, d_ws);
}
```

```cpp
#include <hip/hip_runtime.h>
#include <hip/hip_bf16.h>
#include <stdint.h>

typedef __bf16 bf16x8 __attribute__((ext_vector_type(8)));
typedef __bf16 bf16x4 __attribute__((ext_vector_type(4)));
typedef float f32x4 __attribute__((ext_vector_type(4)));
typedef float f32x2 __attribute__((ext_vector_type(2)));
typedef unsigned u32x4 __attribute__((ext_vector_type(4)));
typedef unsigned u32x2 __attribute__((ext_vector_type(2)));
typedef unsigned u32x10 __attribute__((ext_vector_type(10)));
#define AS3 __attribute__((address_space(3)))
#define AS1 __attribute__((address_space(1)))
#define DI __device__ __forceinline__
#define SB() __builtin_amdgcn_sched_barrier(0)

constexpr int NB = 16, SQ = 96, DM = 640, NH = 32, SK = 1024, DH = 192;
constexpr unsigned SLOT = 12288u, UNIT = 24576u;
constexpr unsigned WAREA = 3u * UNIT;
constexpr unsigned QBUF = 5u * UNIT;
constexpr unsigned MISC = QBUF + 36864u;
constexpr unsigned LDS_BYTES = 163840u;
constexpr unsigned KV_BYTES = (unsigned)NB * NH * SK * DH * 2u;
constexpr unsigned W_BYTES = (unsigned)DM * NH * DH * 2u;
constexpr unsigned X_BYTES = (unsigned)NB * SQ * DM * 2u;
constexpr unsigned PART_BYTES = 122880u;
constexpr unsigned WS_FLAGS = 0u;
constexpr unsigned WS_PART = 65536u;
#define KV0 0
#define KV1 3
#define KV2 6
constexpr unsigned CODE_WARM_BYTES = 38912u;
constexpr float SM_C = 0.07216878364870322f * 1.4426950408889634f;
constexpr float RESC_THR = 6.0f / SM_C;

DI void dma16(unsigned ldsaddr, unsigned voff, __amdgpu_buffer_rsrc_t srd, unsigned soff) {
  asm volatile("s_mov_b32 m0, %0\n\ts_nop 0\n\tbuffer_load_dwordx4 %1, %2, %3 offen lds" :: "s"(ldsaddr), "v"(voff), "s"(srd), "s"(soff) : "memory", "m0");
}
DI void dma16nt(unsigned ldsaddr, unsigned voff, __amdgpu_buffer_rsrc_t srd, unsigned soff) {
  asm volatile("s_mov_b32 m0, %0\n\ts_nop 0\n\tbuffer_load_dwordx4 %1, %2, %3 offen nt lds" :: "s"(ldsaddr), "v"(voff), "s"(srd), "s"(soff) : "memory", "m0");
}
template <int N> DI void waitvm() { asm volatile("s_waitcnt vmcnt(%0)" :: "i"(N) : "memory"); }
DI void wait_vm_dyn(int n) {
  if (n >= 19) waitvm<19>(); else if (n >= 14) waitvm<14>(); else if (n >= 12) waitvm<12>(); else if (n == 11) waitvm<11>(); else if (n >= 6) waitvm<6>(); else if (n == 5) waitvm<5>(); else waitvm<0>();
}
DI void bar() { asm volatile("s_waitcnt lgkmcnt(0)\n\ts_barrier" ::: "memory"); }
DI unsigned lane_id() { unsigned l; asm volatile("v_mbcnt_lo_u32_b32 %0, -1, 0\n\tv_mbcnt_hi_u32_b32 %0, -1, %0" : "=v"(l)); return l; }
DI unsigned rfl(unsigned x) { return __builtin_amdgcn_readfirstlane(x); }
DI bf16x8 ld128(unsigned a) { return *(const AS3 bf16x8*)(size_t)a; }
DI f32x4 ldf4(unsigned a) { return *(const AS3 f32x4*)(size_t)a; }
DI void st128(unsigned a, bf16x8 v) { *(AS3 bf16x8*)(size_t)a = v; }
DI void stu4(unsigned a, u32x4 v) { *(AS3 u32x4*)(size_t)a = v; }
DI void stf4(unsigned a, f32x4 v) { *(AS3 f32x4*)(size_t)a = v; }
DI bf16x4 ldtr(unsigned a) { return __builtin_amdgcn_ds_read_tr16_b64_v4bf16((AS3 bf16x4*)(size_t)a); }
DI bf16x8 cat(bf16x4 lo, bf16x4 hi) { return __builtin_shufflevector(lo, hi, 0, 1, 2, 3, 4, 5, 6, 7); }
DI f32x4 mfma(bf16x8 a, bf16x8 b, f32x4 c) { return __builtin_amdgcn_mfma_f32_16x16x32_bf16(a, b, c, 0, 0, 0); }
DI u32x4 bload(__amdgpu_buffer_rsrc_t srd, unsigned voff, unsigned soff) {
  return __builtin_amdgcn_raw_buffer_load_b128(srd, voff, soff, 0);
}
DI bf16x8 pack8(f32x4 a, f32x4 b) {
  bf16x4 lo = __builtin_convertvector(a, bf16x4), hi = __builtin_convertvector(b, bf16x4);
  return cat(lo, hi);
}
DI f32x4 unpack4(bf16x4 v) {
  const u32x2 u = __builtin_bit_cast(u32x2, v);
  return f32x4{__uint_as_float(u[0] << 16), __uint_as_float(u[0] & 0xffff0000u), __uint_as_float(u[1] << 16), __uint_as_float(u[1] & 0xffff0000u)};
}
DI float max3f(float a, float b, float c) { float r; asm("v_max3_f32 %0, %1, %2, %3" : "=v"(r) : "v"(a), "v"(b), "v"(c)); return r; }
DI float max2f(float a, float b) { float r; asm("v_max_f32_e32 %0, %1, %2" : "=v"(r) : "v"(a), "v"(b)); return r; }
DI float xmax_g(float v) {
  auto a = __builtin_amdgcn_permlane16_swap(__float_as_uint(v), __float_as_uint(v), false, false);
  v = fmaxf(__uint_as_float(a[0]), __uint_as_float(a[1]));
  auto b = __builtin_amdgcn_permlane32_swap(__float_as_uint(v), __float_as_uint(v), false, false);
  return fmaxf(__uint_as_float(b[0]), __uint_as_float(b[1]));
}
DI float xsum_g(float v) {
  auto a = __builtin_amdgcn_permlane16_swap(__float_as_uint(v), __float_as_uint(v), false, false);
  v = __uint_as_float(a[0]) + __uint_as_float(a[1]);
  auto b = __builtin_amdgcn_permlane32_swap(__float_as_uint(v), __float_as_uint(v), false, false);
  return __uint_as_float(b[0]) + __uint_as_float(b[1]);
}
#define STAMP(i) do {} while (0)
#define STAMP_DUMP() do {} while (0)

__global__ __launch_bounds__(512, 2) void mha_kernel(const void* __restrict__ xg, const void* __restrict__ kg, const void* __restrict__ vg,
                                                     const void* __restrict__ wqg, const void* __restrict__ wog, void* __restrict__ outg,
                                                     void* __restrict__ ws) {
  extern __shared__ __attribute__((aligned(16))) char lds_raw[];
  const unsigned ldsb = (unsigned)(size_t)(AS3 char*)lds_raw;
  const int tid = threadIdx.x, lane = tid & 63;
  const int w = __builtin_amdgcn_readfirstlane(tid >> 6);
  const int r = lane & 15, g = lane >> 4;
  const int bid = blockIdx.x, xcd = bid & 7, idx = bid >> 3;
  const int b = idx >> 1, jj = 2 * xcd + (idx & 1), h0 = 2 * jj;
  const unsigned kvbase = (unsigned)(b * NH + h0) * (unsigned)(SK * DH * 2);
  const int lw = w & 3;
  STAMP(0);
  AS1 unsigned* const epw = (AS1 unsigned*)((char*)ws + WS_FLAGS) + (size_t)b * 17 * 32;
  const __amdgpu_buffer_rsrc_t srdP = __builtin_amdgcn_make_buffer_rsrc((char*)ws + WS_PART, 0, 256u * PART_BYTES, 0x00020000);
  const __amdgpu_buffer_rsrc_t srdW = __builtin_amdgcn_make_buffer_rsrc((void*)wqg, 0, W_BYTES, 0x00020000);
  const __amdgpu_buffer_rsrc_t srdX = __builtin_amdgcn_make_buffer_rsrc((void*)xg, 0, X_BYTES, 0x00020000);
  const __amdgpu_buffer_rsrc_t srdO = __builtin_amdgcn_make_buffer_rsrc((void*)wog, 0, W_BYTES, 0x00020000);
  const unsigned xbase = (unsigned)b * (unsigned)(SQ * DM * 2);
  auto obuf_addr = [&](int c) -> unsigned { const int k = c % 6; return k < 2 ? WAREA + (unsigned)k * UNIT : (k < 5 ? (unsigned)(k - 2) * UNIT : QBUF); };

  u32x4 cw = {0u, 0u, 0u, 0u};
  if (w == 4 || w == 5) {
    const char* pc0 = (const char*)__builtin_amdgcn_s_getpc();
    const unsigned off = (unsigned)idx * 2048u + (unsigned)(w - 4) * 1024u + lane * 16u;
    if (off + 16u <= CODE_WARM_BYTES) cw = *(const u32x4*)(pc0 + off);
  }
  if (w < 4) {
    const int rh = w & 1, kh = w >> 1;
    {
      unsigned voW[3], voX[3];
#pragma unroll
      for (int i = 0; i < 3; ++i) {
        const unsigned p = 64u * (3u * lw + i) + lane;
        const unsigned row = p / 24u, c = p % 24u;
        voW[i] = row * 12288u + ((c ^ ((((row >> 1) & 1u) | (((row >> 3) & 1u) << 1)) << 1)) << 4);
        const unsigned rx = p >> 3, cx = p & 7u;
        voX[i] = rx * 1280u + ((cx ^ ((rx >> 1) & 7u)) << 4);
      }
      const unsigned stw = ldsb + 3072u * lw + lane * 16u;
      const unsigned wq0 = (unsigned)h0 * 384u;
      constexpr int QD = 4;
      u32x4 R[QD][9];
      auto ldc = [&](int s, int c) {
        const unsigned wo = (unsigned)(64 * c) * 12288u + wq0;
#pragma unroll
        for (int i = 0; i < 3; ++i) R[s][i] = bload(srdW, voW[i], wo);
#pragma unroll
        for (int i = 0; i < 3; ++i) R[s][3 + i] = bload(srdW, voW[i], wo + 32u * 12288u);
#pragma unroll
        for (int i = 0; i < 3; ++i) R[s][6 + i] = bload(srdX, voX[i], xbase + (unsigned)c * 128u);
      };
      auto stc = [&](int s, int c) {
        const unsigned base = stw + ((c & 1) ? WAREA : QBUF);
#pragma unroll
        for (int j = 0; j < 3; ++j)
#pragma unroll
          for (int i = 0; i < 3; ++i) stu4(base + (unsigned)j * SLOT + 1024u * i, R[s][3 * j + i]);
      };
      ldc(0, 0); ldc(1, 1); ldc(2, 2);
      u32x4 wd[4], we[8];
      SB();
      STAMP(104);
      stc(0, 0);
      STAMP(105);
      ldc(3, 3); ldc(0, 4);
      bar();
      STAMP(106);
#pragma unroll
      for (int c = 0; c < 9; ++c) {
        stc((c + 1) % QD, c + 1);
        if (c + 1 + QD <= 9) ldc((c + 1) % QD, c + 1 + QD);
        if (c == 4) {
#pragma unroll
          for (int i = 0; i < 4; ++i) {
            const unsigned e = 60u * i + (lane < 60 ? lane : 59);
            const unsigned row = (unsigned)(40 * b + 10 * lw) + e / 24u;
            wd[i] = bload(srdW, row * 12288u + (e % 24u) * 16u, (unsigned)(h0 + 1) * 384u);
          }
          const unsigned so = (unsigned)(h0 * 192 + 24 * b) * 1280u + (unsigned)lw * 7680u;
#pragma unroll
          for (int i = 0; i < 8; ++i) we[i] = bload(srdO, (i < 7 || lane < 32 ? lane : 31) * 16u, so + 1024u * i);
        }
        STAMP(107 + c);
        bar();
      }
#pragma unroll
      for (int i = 0; i < 4; ++i) asm volatile("" :: "v"(wd[i]));
#pragma unroll
      for (int i = 0; i < 8; ++i) asm volatile("" :: "v"(we[i]));
    }
    STAMP(1);
    const unsigned ln = lane_id();
    const unsigned r2 = ln & 15u, g2 = ln >> 4;
    const unsigned fk = (r2 >> 1) & 7;
    const unsigned ka0 = ldsb + kh * SLOT + 384u * r2 + ((g2 ^ fk) << 4);
    const unsigned ka1 = ldsb + kh * SLOT + 384u * r2 + (((4u + g2) ^ fk) << 4);
    const unsigned qa = ldsb + QBUF + (unsigned)rh * 18432u + ln * 16u;
    const unsigned qq = r2 >> 2, pp = r2 & 3;
    const unsigned fv = (2u * (g2 & 1) + (qq >> 1)) << 1;
    unsigned va[4];
#pragma unroll
    for (int d = 0; d < 4; ++d) va[d] = ldsb + kh * SLOT + 384u * (4u * g2 + qq) + (((2u * d + (pp >> 1)) ^ fv) << 4) + 8u * (pp & 1);

#pragma unroll
    for (int pr = 0; pr < 2; ++pr) {
      f32x4 O[12][3];
#pragma unroll
      for (int d = 0; d < 12; ++d)
#pragma unroll
        for (int q = 0; q < 3; ++q) O[d][q] = f32x4{0.f, 0.f, 0.f, 0.f};
      float mrun[3] = {-INFINITY, -INFINITY, -INFINITY};
      f32x2 lsum2[3] = {f32x2{0.f, 0.f}, f32x2{0.f, 0.f}, f32x2{0.f, 0.f}};
      unsigned kpos = (unsigned)((32 * pr) % 3) * UNIT;
      for (int t = 0; t < 16; ++t) {
        const unsigned vpos = kpos == 2u * UNIT ? 0u : kpos + UNIT;
        f32x4 S[2][3];
        {
          bf16x8 kf[3][2], qf[3][3];
          auto ldq = [&](int s) {
#pragma unroll
            for (int q = 0; q < 3; ++q) qf[s % 3][q] = ld128(qa + (unsigned)(q * 6 + s) * 1024u);
          };
          const unsigned k0 = ka0 + kpos, k1 = ka1 + kpos;
          auto ldk = [&](int s) {
            const unsigned kb = ((s & 1) ? k1 : k0) + 128u * (s >> 1);
            kf[s % 3][0] = ld128(kb); kf[s % 3][1] = ld128(kb + 6144u);
          };
          if (t > 0) { ldq(0); ldq(1); }
          asm volatile("s_barrier" ::: "memory");
          STAMP(2 + 3 * (pr * 16 + t));
          if (t == 0) { ldq(0); ldq(1); }
          ldk(0); ldk(1);
#pragma unroll
          for (int q = 0; q < 3; ++q) { S[0][q] = f32x4{0.f, 0.f, 0.f, 0.f}; S[1][q] = f32x4{0.f, 0.f, 0.f, 0.f}; }
#pragma unroll
          for (int s = 0; s < 6; ++s) {
            SB();
            if (s + 2 < 6) { ldk(s + 2); ldq(s + 2); }
            SB();
#pragma unroll
            for (int q = 0; q < 3; ++q) {
              S[0][q] = mfma(kf[s % 3][0], qf[s % 3][q], S[0][q]);
              S[1][q] = mfma(kf[s % 3][1], qf[s % 3][q], S[1][q]);
            }
          }
          SB();
        }
        bar();
        STAMP(4 + 3 * (pr * 16 + t));
        bf16x8 vf[7];
        auto ldv = [&](int d) {
          const unsigned a = va[d & 3] + vpos + 128u * (d >> 2);
          vf[d % 7] = cat(ldtr(a), ldtr(a + 6144u));
        };
        ldv(0); ldv(1); ldv(2); ldv(3); ldv(4); ldv(5);
        SB();
        float mx[3];
        bool need = false;
#pragma unroll
        for (int q = 0; q < 3; ++q) {
          mx[q] = max3f(max3f(S[0][q][0], S[0][q][1], S[0][q][2]), max3f(S[0][q][3], S[1][q][0], S[1][q][1]), max2f(S[1][q][2], S[1][q][3]));
          need |= (mx[q] > mrun[q] + RESC_THR);
        }
        if (__any(need)) {
#pragma unroll
          for (int q = 0; q < 3; ++q) {
            const float mn = fmaxf(mrun[q], xmax_g(mx[q]));
            const float al = __builtin_amdgcn_exp2f((mrun[q] - mn) * SM_C);
            mrun[q] = mn;
            lsum2[q] *= al;
#pragma unroll
            for (int d = 0; d < 12; ++d) O[d][q] *= al;
          }
        }
        bf16x8 pf[3];
#pragma unroll
        for (int q = 0; q < 3; ++q) {
          const float nm = -mrun[q] * SM_C;
          const f32x2 nm2 = {nm, nm}, c2 = {SM_C, SM_C};
          f32x2 e[4];
#pragma unroll
          for (int i = 0; i < 4; ++i) {
            const f32x2 sv = {S[i >> 1][q][2 * (i & 1)], S[i >> 1][q][2 * (i & 1) + 1]};
            const f32x2 a = sv * c2 + nm2;
            e[i] = f32x2{__builtin_amdgcn_exp2f(a[0]), __builtin_amdgcn_exp2f(a[1])};
          }
          lsum2[q] += (e[0] + e[1]) + (e[2] + e[3]);
          pf[q] = pack8(f32x4{e[0][0], e[0][1], e[1][0], e[1][1]}, f32x4{e[2][0], e[2][1], e[3][0], e[3][1]});
        }
        STAMP(3 + 3 * (pr * 16 + t));
#pragma unroll
        for (int d = 0; d < 12; ++d) {
          SB();
          if (d + 6 < 12) ldv(d + 6);
          SB();
#pragma unroll
          for (int q = 0; q < 3; ++q) O[d][q] = mfma(vf[d % 7], pf[q], O[d][q]);
        }
        SB();
        asm volatile("s_waitcnt lgkmcnt(0)" ::: "memory");
        kpos = vpos == 2u * UNIT ? 0u : vpos + UNIT;
      }
      float lsum[3];
      {
        const unsigned sa = ldsb + MISC + (unsigned)w * 384u + r2 * 8u;
#pragma unroll
        for (int q = 0; q < 3; ++q) {
          lsum[q] = xsum_g(lsum2[q][0] + lsum2[q][1]);
          if (g2 == 0) *(AS3 f32x2*)(size_t)(sa + q * 128u) = f32x2{mrun[q], lsum[q]};
        }
      }
      bar();
      float sc[3];
      {
        const unsigned sb = ldsb + MISC + (unsigned)(w ^ 2) * 384u + r2 * 8u;
#pragma unroll
        for (int q = 0; q < 3; ++q) {
          const f32x2 o = *(const AS3 f32x2*)(size_t)(sb + q * 128u);
          const float mn = fmaxf(mrun[q], o[0]);
          const float a0 = __builtin_amdgcn_exp2f((mrun[q] - mn) * SM_C), a1 = __builtin_amdgcn_exp2f((o[0] - mn) * SM_C);
          const float l = lsum[q] * a0 + o[1] * a1;
          sc[q] = a0 / l;
        }
      }
      const unsigned xreg = pr == 0 ? WAREA : 0u;
      const unsigned xw = ldsb + xreg + (unsigned)w * 18432u + ln * 16u;
      const unsigned xr = ldsb + xreg + (unsigned)(w ^ 2) * 18432u + ln * 16u;
      if (kh) {
#pragma unroll
        for (int d = 0; d < 6; ++d)
#pragma unroll
          for (int q = 0; q < 3; ++q) { stf4(xw + (unsigned)(d * 3 + q) * 1024u, O[d][q] * sc[q]); O[d][q] = O[6 + d][q] * sc[q]; }
      } else {
#pragma unroll
        for (int d = 0; d < 6; ++d)
#pragma unroll
          for (int q = 0; q < 3; ++q) { stf4(xw + (unsigned)(d * 3 + q) * 1024u, O[6 + d][q] * sc[q]); O[d][q] = O[d][q] * sc[q]; }
      }
      bar();
#pragma unroll
      for (int d = 0; d < 6; ++d)
#pragma unroll
        for (int q = 0; q < 3; ++q) O[d][q] += ldf4(xr + (unsigned)(d * 3 + q) * 1024u);
      asm volatile("s_waitcnt lgkmcnt(0)" ::: "memory");
#pragma unroll
      for (int q = 0; q < 3; ++q)
#pragma unroll
        for (int s = 0; s < 3; ++s) st128(xr + (unsigned)(q * 3 + s) * 1024u, pack8(O[2 * s][q], O[2 * s + 1][q]));
      bar();
      STAMP(100 + pr);
      bar();
    }
    {
      const unsigned l4 = lane_id();
      const unsigned fo = ((l4 >> 4) & 3u) << 1;
      const unsigned voO = (l4 >> 3) * 1280u + (((l4 & 7u) ^ fo) << 4);
      const unsigned wob = (unsigned)(h0 + 1) * 192u * 1280u + (unsigned)lw * 48u * 1280u;
      auto issue = [&](int c) {
        const unsigned la = ldsb + obuf_addr(c) + (unsigned)lw * 6144u;
#pragma unroll
        for (int i = 0; i < 6; ++i) dma16(rfl(la + 1024u * i), voO, srdO, rfl(wob + (unsigned)i * 10240u + (unsigned)c * 128u));
      };
      issue(2); issue(3); issue(4); issue(5);
      for (int c = 0; c < 10; ++c) {
        if (c >= 1) {
          if (c <= 5) waitvm<18>(); else if (c == 6) waitvm<12>(); else if (c == 7) waitvm<6>(); else waitvm<0>();
        }
        bar();
        if (c >= 1 && c + 5 <= 9) issue(c + 5);
      }
    }
  } else {
    const int hw = w - 4, rh = hw & 1, xh = hw >> 1;
    __builtin_amdgcn_s_setprio(1);
    const __amdgpu_buffer_rsrc_t srdK = __builtin_amdgcn_make_buffer_rsrc((void*)kg, 0, KV_BYTES, 0x00020000);
    const __amdgpu_buffer_rsrc_t srdV = __builtin_amdgcn_make_buffer_rsrc((void*)vg, 0, KV_BYTES, 0x00020000);
    unsigned voK[3], voV[3];
#pragma unroll
    for (int i = 0; i < 3; ++i) {
      const unsigned p = 64u * i + lane;
      const unsigned row = p / 24u, c = p % 24u;
      voK[i] = (row + 16u * hw) * 384u + ((c ^ ((row >> 1) & 7u)) << 4);
      voV[i] = (row + 16u * hw) * 384u + ((c ^ (((row >> 1) & 3u) << 1)) << 4);
    }
    const unsigned ldsw = ldsb + 6144u * hw;
    auto issueKV = [&](int u) {
      const unsigned pos = ldsw + (unsigned)(u % 3) * UNIT;
      const unsigned go = kvbase + (unsigned)(u >> 1) * UNIT;
      if (u & 1) {
#pragma unroll
        for (int i = 0; i < 3; ++i) dma16nt(rfl(pos + 1024u * i), voV[i], srdV, rfl(go));
#pragma unroll
        for (int i = 0; i < 3; ++i) dma16nt(rfl(pos + 3072u + 1024u * i), voV[i], srdV, rfl(go + 3072u));
      } else {
#pragma unroll
        for (int i = 0; i < 3; ++i) dma16nt(rfl(pos + 1024u * i), voK[i], srdK, rfl(go));
#pragma unroll
        for (int i = 0; i < 3; ++i) dma16nt(rfl(pos + 3072u + 1024u * i), voK[i] ^ 64u, srdK, rfl(go + 3072u));
      }
    };
    {
      const unsigned dst = rfl(ldsb + 2u * UNIT + 1024u * hw);
      const unsigned so = (unsigned)idx * 61440u + (unsigned)hw * 15360u;
#pragma unroll
      for (int i = 0; i < 15; ++i) dma16(dst, lane * 16u, srdX, rfl(so + 1024u * i));
#pragma unroll
      for (int i = 0; i < 4; ++i) {
        const unsigned e = 60u * i + (lane < 60 ? lane : 59);
        const unsigned row = (unsigned)(40 * b + 10 * hw) + e / 24u;
        dma16(dst, row * 12288u + (e % 24u) * 16u, srdW, rfl((unsigned)h0 * 384u));
      }
    }
    const unsigned qq = r >> 2, pp = r & 3;
    const unsigned fw = ((qq >> 1) | (((unsigned)g & 1u) << 1)) << 1;
    unsigned wa[6];
#pragma unroll
    for (int d = 0; d < 6; ++d) {
      const unsigned dg = 6u * xh + d;
      wa[d] = 384u * (8u * g + qq) + (((2u * (dg & 3u) + (pp >> 1)) ^ fw) << 4) + 8u * (pp & 1) + 128u * (dg >> 2);
    }
    asm volatile("" :: "v"(cw));
    unsigned E = rfl(__hip_atomic_load(epw, __ATOMIC_RELAXED, __HIP_MEMORY_SCOPE_AGENT));
    asm volatile("" : "+s"(E));
    f32x4 QA[6][3];
    auto zeroQA = [&]() {
#pragma unroll
      for (int d = 0; d < 6; ++d)
#pragma unroll
        for (int q = 0; q < 3; ++q) QA[d][q] = f32x4{0.f, 0.f, 0.f, 0.f};
    };
    auto writeQ = [&]() {
      const unsigned l = lane_id(), rr = l & 15u, gg = l >> 4;
      const unsigned qw = ldsb + QBUF + (unsigned)rh * 18432u + (rr + 16u * (gg >> 1)) * 16u + 8u * (gg & 1u) + (unsigned)xh * 3072u;
#pragma unroll
      for (int q = 0; q < 3; ++q)
#pragma unroll
        for (int d = 0; d < 6; ++d)
          *(AS3 bf16x4*)(size_t)(qw + (unsigned)(q * 6 + (d >> 1)) * 1024u + 512u * (d & 1)) = __builtin_convertvector(QA[d][q], bf16x4);
    };
    auto qload = [&](unsigned wb, bf16x8 (&af)[6]) {
#pragma unroll
      for (int i = 0; i < 6; ++i) { const unsigned ad = wb + wa[i]; af[i] = cat(ldtr(ad), ldtr(ad + 1536u)); }
    };
    auto qmma = [&](const bf16x8 (&af)[6], const bf16x8 (&xf)[3]) {
#pragma unroll
      for (int i = 0; i < 6; ++i)
#pragma unroll
        for (int q = 0; q < 3; ++q) QA[i][q] = mfma(af[i], xf[q], QA[i][q]);
    };
    zeroQA();
    {
      const unsigned fk = (r >> 1) & 7;
      const unsigned xa0 = 128u * (48u * rh + r) + (((unsigned)g ^ fk) << 4);
      const unsigned xa1 = 128u * (48u * rh + r) + (((4u + g) ^ fk) << 4);
      for (int c = 0; c < 10; ++c) {
        bar();
        STAMP(98 + c);
        if (c == KV0) issueKV(0); else if (c == KV1) issueKV(1); else if (c == KV2) issueKV(2);
        const unsigned base = ldsb + ((c & 1) ? WAREA : QBUF);
        bf16x8 xf0[3], xf1[3], af0[6], af1[6];
#pragma unroll
        for (int q = 0; q < 3; ++q) xf0[q] = ld128(base + 2u * SLOT + xa0 + 2048u * q);
        qload(base, af0);
        SB();
#pragma unroll
        for (int q = 0; q < 3; ++q) xf1[q] = ld128(base + 2u * SLOT + xa1 + 2048u * q);
#pragma unroll
        for (int i = 0; i < 6; ++i) {
          SB();
          { const unsigned ad = base + SLOT + wa[i]; af1[i] = cat(ldtr(ad), ldtr(ad + 1536u)); }
          SB();
#pragma unroll
          for (int q = 0; q < 3; ++q) QA[i][q] = mfma(af0[i], xf0[q], QA[i][q]);
        }
        SB();
        qmma(af1, xf1);
        SB();
      }
    }
    writeQ();
    zeroQA();
    {
      unsigned voW[3], voX0, voX1;
#pragma unroll
      for (int i = 0; i < 3; ++i) {
        const unsigned p = 64u * (3u * hw + i) + lane;
        const unsigned row = p / 24u, c = p % 24u;
        voW[i] = row * 12288u + ((c ^ ((((row >> 1) & 1u) | (((row >> 3) & 1u) << 1)) << 1)) << 4);
      }
      {
        const unsigned p0 = 96u * hw + lane, p1 = p0 + 64u;
        voX0 = (p0 >> 2) * 1280u + (((p0 & 3u) ^ ((0u - (p0 >> 4)) & 3u)) << 4);
        voX1 = (p1 >> 2) * 1280u + (((p1 & 3u) ^ ((0u - (p1 >> 4)) & 3u)) << 4);
      }
      const unsigned xra = 64u * (48u * rh + r) + ((((unsigned)g) ^ ((0u - ((unsigned)r >> 2)) & 3u)) << 4);
      const unsigned wq1 = (unsigned)(h0 + 1) * 384u;
      int prev = 12;
      int jn = 0, jc = 0;
      for (int h = 0; h < 32; ++h) {
        wait_vm_dyn(prev);
        STAMP(1 + h);
        bar();
        int cnt = 0;
        const int hm = h % 3;
        const bool docomp = (h >= 2 && hm != 1 && jc < 20);
        bf16x8 xf[3], af[6];
        if (docomp) {
          const unsigned wbuf = ldsb + WAREA + (unsigned)(jc & 1) * 18432u;
#pragma unroll
          for (int q = 0; q < 3; ++q) xf[q] = ld128(wbuf + SLOT + xra + 1024u * q);
          qload(wbuf, af);
        }
        SB();
        if (hm != 2 && jn < 20) {
          const unsigned wbuf = ldsb + WAREA + (unsigned)(jn & 1) * 18432u;
          const unsigned so = (unsigned)(32 * jn) * 12288u + wq1;
#pragma unroll
          for (int i = 0; i < 3; ++i) dma16(rfl(wbuf + 3072u * hw + 1024u * i), voW[i], srdW, rfl(so));
          const unsigned xo = xbase + (unsigned)jn * 64u;
          dma16(rfl(wbuf + SLOT + 1536u * hw), voX0, srdX, rfl(xo));
          if (lane < 32) dma16(rfl(wbuf + SLOT + 1536u * hw + 1024u), voX1, srdX, rfl(xo));
          ++jn; cnt += 5;
        }
        if (h >= 1) { issueKV(h + 2); }
        cnt += 6;
        if (false && h == 26) {
          const unsigned so = (unsigned)(h0 * 192 + 24 * b) * 1280u + (unsigned)hw * 7680u;
#pragma unroll
          for (int i = 0; i < 7; ++i) dma16(rfl(ldsb + WAREA + 36864u + 3072u * hw), lane * 16u, srdO, rfl(so + 1024u * i));
          if (lane < 32) dma16(rfl(ldsb + WAREA + 36864u + 3072u * hw), lane * 16u, srdO, rfl(so + 7168u));
          cnt += 8;
        }
        SB();
        if (docomp) { qmma(af, xf); ++jc; }
        SB();
        prev = cnt;
      }
    }
    bar();
    issueKV(34);
    bar(); bar();
    bf16x8 Of[6][3];
    auto readO = [&](unsigned xreg) {
#pragma unroll
      for (int k2 = 0; k2 < 2; ++k2) {
        const unsigned src = ldsb + xreg + (unsigned)(rh + 2 * (1 - k2)) * 18432u + lane_id() * 16u;
#pragma unroll
        for (int q = 0; q < 3; ++q)
#pragma unroll
          for (int s = 0; s < 3; ++s) Of[3 * k2 + s][q] = ld128(src + (unsigned)(q * 3 + s) * 1024u);
      }
    };
    readO(WAREA);
    bar();
    writeQ();
    const unsigned l3 = lane_id(), g3 = l3 >> 4, qq3 = (l3 & 15u) >> 2, pp3 = l3 & 3u;
    const unsigned fv = (2u * (g3 & 1) + (qq3 >> 1)) << 1;
    unsigned oa[2];
#pragma unroll
    for (int nb = 0; nb < 2; ++nb) oa[nb] = ldsb + 128u * (4u * g3 + qq3) + (((4u * xh + 2u * nb + (pp3 >> 1)) ^ fv) << 4) + 8u * (pp3 & 1);
    const unsigned fo = ((l3 >> 4) & 3u) << 1;
    const unsigned voO = (l3 >> 3) * 1280u + (((l3 & 7u) ^ fo) << 4);
    auto oload = [&](unsigned cb, int nb, bf16x8 (&af)[6]) {
#pragma unroll
      for (int ks = 0; ks < 6; ++ks) { const unsigned ad = oa[nb] + cb + 4096u * ks; af[ks] = cat(ldtr(ad), ldtr(ad + 2048u)); }
    };
    auto omma = [&](const bf16x8 (&af)[6], f32x4 (&acc)[3]) {
#pragma unroll
      for (int ks = 0; ks < 6; ++ks)
#pragma unroll
        for (int q = 0; q < 3; ++q) acc[q] = mfma(af[ks], Of[ks][q], acc[q]);
    };
    u32x10 PP[2][3][2];
#pragma unroll
    for (int nb = 0; nb < 2; ++nb)
#pragma unroll
      for (int q = 0; q < 3; ++q)
#pragma unroll
        for (int d = 0; d < 2; ++d)
#pragma unroll
          for (int c = 0; c < 10; ++c) PP[nb][q][d][c] = 0u;
    {
      const unsigned wob0 = (unsigned)h0 * 192u * 1280u + (unsigned)hw * 48u * 1280u;
      auto half = [&](int h, int nb, int cc, bool comp, int ci, int prev) -> int {
        wait_vm_dyn(prev);
        STAMP(33 + h);
        bar();
        bf16x8 af[4];
        const unsigned oab = oa[nb] + WAREA + (unsigned)(cc & 1) * UNIT;
        auto ldo = [&](int ks) { af[ks & 3] = cat(ldtr(oab + 4096u * ks), ldtr(oab + 4096u * ks + 2048u)); };
        if (comp) { ldo(0); ldo(1); ldo(2); ldo(3); }
        SB();
        int cnt = 0;
        if (ci >= 0) {
          const unsigned wob = wob0 + (ci >= 10 ? 192u * 1280u + (unsigned)(ci - 10) * 128u : (unsigned)ci * 128u);
#pragma unroll
          for (int i = 0; i < 6; ++i) dma16(rfl(ldsb + WAREA + (unsigned)(ci & 1) * UNIT + 6144u * hw + 1024u * i), voO, srdO, rfl(wob + (unsigned)i * 10240u));
          cnt += 6;
        }
        if (h >= 1 && 34 + h < 64) issueKV(34 + h);
        if (34 + h < 64) cnt += 6;
        SB();
        if (comp) {
          f32x4 acc[3] = {f32x4{0.f, 0.f, 0.f, 0.f}, f32x4{0.f, 0.f, 0.f, 0.f}, f32x4{0.f, 0.f, 0.f, 0.f}};
#pragma unroll
          for (int ks = 0; ks < 6; ++ks) {
#pragma unroll
            for (int q = 0; q < 3; ++q) acc[q] = mfma(af[ks & 3], Of[ks][q], acc[q]);
            SB();
            if (ks + 4 < 6) ldo(ks + 4);
            SB();
          }
#pragma unroll
          for (int q = 0; q < 3; ++q) {
            const u32x2 pk = __builtin_bit_cast(u32x2, __builtin_convertvector(acc[q], bf16x4));
            PP[nb][q][0][cc] = pk[0]; PP[nb][q][1][cc] = pk[1];
          }
        }
        SB();
        STAMP(65 + h);
        return cnt;
      };
      int prev = 12;
      for (int i = 0; i < 16; ++i) {
        prev = half(2 * i, 1, i - 2, i >= 2 && i <= 11, -1, prev);
        prev = half(2 * i + 1, 0, i - 1, i >= 1 && i <= 10, i < 12 ? i : -1, prev);
      }
    }
    waitvm<0>();
    bar(); bar(); bar();
    readO(0u);
    bar();
    {
      const unsigned pbase = (unsigned)bid * PART_BYTES + (unsigned)hw * 30720u;
      const unsigned l16 = lane_id() * 16u;
      bf16x8 af[6];
      bar();
      auto ldstep = [&](unsigned cb, int st) {
        const unsigned ad = oa[st / 6] + cb + 4096u * (st % 6);
        af[st % 6] = cat(ldtr(ad), ldtr(ad + 2048u));
      };
#pragma unroll
      for (int st = 0; st < 5; ++st) ldstep(obuf_addr(0), st);
      for (int c = 0; c < 10; ++c) {
        STAMP(108 + c);
        const unsigned cb = obuf_addr(c), cn = obuf_addr(c + 1);
        bf16x4 pk[2][3];
#pragma unroll
        for (int nb = 0; nb < 2; ++nb) {
          f32x4 acc[3];
#pragma unroll
          for (int q = 0; q < 3; ++q) {
            const unsigned u0 = PP[nb][q][0][c], u1 = PP[nb][q][1][c];
            acc[q] = f32x4{__uint_as_float(u0 << 16), __uint_as_float(u0 & 0xffff0000u), __uint_as_float(u1 << 16), __uint_as_float(u1 & 0xffff0000u)};
          }
#pragma unroll
          for (int ks = 0; ks < 6; ++ks) {
            const int st = nb * 6 + ks;
            SB();
            if (st + 5 < 12) ldstep(cb, st + 5);
            else if (c < 9) ldstep(cn, st + 5 - 12);
            SB();
#pragma unroll
            for (int q = 0; q < 3; ++q) acc[q] = mfma(af[ks], Of[ks][q], acc[q]);
          }
          SB();
#pragma unroll
          for (int q = 0; q < 3; ++q) pk[nb][q] = __builtin_convertvector(acc[q], bf16x4);
        }
#pragma unroll
        for (int q = 0; q < 3; ++q)
          __builtin_amdgcn_raw_buffer_store_b128(__builtin_bit_cast(u32x4, cat(pk[0][q], pk[1][q])), srdP, l16,
                                                 pbase + (unsigned)(c * 3 + q) * 1024u, 16  );
        if (c < 9) bar();
      }
      waitvm<0>();
    }
    bar();
    const unsigned tag = E + 1u;
    if (hw == 0) {
      const unsigned l5 = lane_id();
      if (l5 == 0) __hip_atomic_store(epw + 32 * (1 + jj), tag, __ATOMIC_RELAXED, __HIP_MEMORY_SCOPE_AGENT);
      for (unsigned spin = 0; spin < (1u << 22); ++spin) {
        const unsigned v = __hip_atomic_load(epw + 32 * (1 + (l5 & 15)), __ATOMIC_RELAXED, __HIP_MEMORY_SCOPE_AGENT);
        if (__all(v == tag)) break;
        __builtin_amdgcn_s_sleep(1);
      }
      if (jj == 0 && l5 == 0) __hip_atomic_store(epw, tag, __ATOMIC_RELAXED, __HIP_MEMORY_SCOPE_AGENT);
      __builtin_amdgcn_fence(__ATOMIC_ACQUIRE, "wavefront");
    }
  }
  if (w < 4) bar();
  bar();
  STAMP(126);
  {
    const int piece = jj * 8 + w;
    const unsigned l6 = lane_id(), r6 = l6 & 15u, g6 = l6 >> 4;
    if (piece < 120) {
      float s[8];
#pragma unroll
      for (int i = 0; i < 8; ++i) s[i] = 0.f;
      u32x4 v[16];
#pragma unroll
      for (int k = 0; k < 16; ++k) {
        const unsigned obid = (unsigned)(((b * 2 + (k & 1)) << 3) | (k >> 1));
        v[k] = __builtin_amdgcn_raw_buffer_load_b128(srdP, l6 * 16u, obid * PART_BYTES + (unsigned)piece * 1024u, 16  );
      }
#pragma unroll
      for (int k = 0; k < 16; ++k)
#pragma unroll
        for (int i = 0; i < 4; ++i) { s[2 * i] += __uint_as_float(v[k][i] << 16); s[2 * i + 1] += __uint_as_float(v[k][i] & 0xffff0000u); }
      const int hp = piece / 30, rem = piece % 30, c = rem / 3, q = rem % 3;
      const int row = 48 * (hp & 1) + 16 * q + (int)r6, n0 = 64 * c + 32 * (hp >> 1) + 4 * (int)g6;
      __bf16* op = (__bf16*)outg + ((size_t)b * SQ + row) * DM + n0;
      *(bf16x4*)op = __builtin_convertvector((f32x4{s[0], s[1], s[2], s[3]}), bf16x4);
      *(bf16x4*)(op + 16) = __builtin_convertvector((f32x4{s[4], s[5], s[6], s[7]}), bf16x4);
    }
  }
  STAMP(127);
  STAMP_DUMP();
}

extern "C" void kernel_launch(void* const* d_in, const int* in_sizes, int n_in, void* d_out, int out_size, void* d_ws, size_t ws_size,
                              hipStream_t stream) {
  (void)in_sizes; (void)n_in; (void)out_size; (void)ws_size;
  (void)hipFuncSetAttribute((const void*)mha_kernel, hipFuncAttributeMaxDynamicSharedMemorySize, LDS_BYTES);
  mha_kernel<<<256, 512, LDS_BYTES, stream>>>(d_in[0], d_in[1], d_in[2], d_in[3], d_in[4], d_out, d_ws);
}
```

```cpp
#include <hip/hip_runtime.h>
#include <hip/hip_bf16.h>
#include <stdint.h>

typedef __bf16 bf16x8 __attribute__((ext_vector_type(8)));
typedef __bf16 bf16x4 __attribute__((ext_vector_type(4)));
typedef float f32x4 __attribute__((ext_vector_type(4)));
typedef float f32x2 __attribute__((ext_vector_type(2)));
typedef unsigned u32x4 __attribute__((ext_vector_type(4)));
typedef unsigned u32x2 __attribute__((ext_vector_type(2)));
typedef unsigned u32x10 __attribute__((ext_vector_type(10)));
#define AS3 __attribute__((address_space(3)))
#define AS1 __attribute__((address_space(1)))
#define DI __device__ __forceinline__
#define SB() __builtin_amdgcn_sched_barrier(0)

constexpr int NB = 16, SQ = 96, DM = 640, NH = 32, SK = 1024, DH = 192;
constexpr unsigned SLOT = 12288u, UNIT = 24576u;
constexpr unsigned WAREA = 3u * UNIT;
constexpr unsigned QBUF = 5u * UNIT;
constexpr unsigned MISC = QBUF + 36864u;
constexpr unsigned LDS_BYTES = 163840u;
constexpr unsigned KV_BYTES = (unsigned)NB * NH * SK * DH * 2u;
constexpr unsigned W_BYTES = (unsigned)DM * NH * DH * 2u;
constexpr unsigned X_BYTES = (unsigned)NB * SQ * DM * 2u;
constexpr unsigned PART_BYTES = 122880u;
constexpr unsigned WS_FLAGS = 0u;
constexpr unsigned WS_PART = 65536u;
#define WARMC 4
#define KV0 0
#define KV1 3
#define KV2 6
constexpr unsigned CODE_WARM_BYTES = 38912u;
constexpr float SM_C = 0.07216878364870322f * 1.4426950408889634f;
constexpr float RESC_THR = 6.0f / SM_C;

DI void dma16(unsigned ldsaddr, unsigned voff, __amdgpu_buffer_rsrc_t srd, unsigned soff) {
  asm volatile("s_mov_b32 m0, %0\n\ts_nop 0\n\tbuffer_load_dwordx4 %1, %2, %3 offen lds" :: "s"(ldsaddr), "v"(voff), "s"(srd), "s"(soff) : "memory", "m0");
}
DI void dma16nt(unsigned ldsaddr, unsigned voff, __amdgpu_buffer_rsrc_t srd, unsigned soff) {
  asm volatile("s_mov_b32 m0, %0\n\ts_nop 0\n\tbuffer_load_dwordx4 %1, %2, %3 offen nt lds" :: "s"(ldsaddr), "v"(voff), "s"(srd), "s"(soff) : "memory", "m0");
}
template <int N> DI void waitvm() { asm volatile("s_waitcnt vmcnt(%0)" :: "i"(N) : "memory"); }
DI void wait_vm_dyn(int n) {
  if (n >= 19) waitvm<19>(); else if (n >= 14) waitvm<14>(); else if (n >= 12) waitvm<12>(); else if (n == 11) waitvm<11>(); else if (n >= 6) waitvm<6>(); else if (n == 5) waitvm<5>(); else waitvm<0>();
}
DI void bar() { asm volatile("s_waitcnt lgkmcnt(0)\n\ts_barrier" ::: "memory"); }
DI unsigned lane_id() { unsigned l; asm volatile("v_mbcnt_lo_u32_b32 %0, -1, 0\n\tv_mbcnt_hi_u32_b32 %0, -1, %0" : "=v"(l)); return l; }
DI unsigned rfl(unsigned x) { return __builtin_amdgcn_readfirstlane(x); }
DI bf16x8 ld128(unsigned a) { return *(const AS3 bf16x8*)(size_t)a; }
DI f32x4 ldf4(unsigned a) { return *(const AS3 f32x4*)(size_t)a; }
DI void st128(unsigned a, bf16x8 v) { *(AS3 bf16x8*)(size_t)a = v; }
DI void stu4(unsigned a, u32x4 v) { *(AS3 u32x4*)(size_t)a = v; }
DI void stf4(unsigned a, f32x4 v) { *(AS3 f32x4*)(size_t)a = v; }
DI bf16x4 ldtr(unsigned a) { return __builtin_amdgcn_ds_read_tr16_b64_v4bf16((AS3 bf16x4*)(size_t)a); }
DI bf16x8 cat(bf16x4 lo, bf16x4 hi) { return __builtin_shufflevector(lo, hi, 0, 1, 2, 3, 4, 5, 6, 7); }
DI f32x4 mfma(bf16x8 a, bf16x8 b, f32x4 c) { return __builtin_amdgcn_mfma_f32_16x16x32_bf16(a, b, c, 0, 0, 0); }
DI u32x4 bload(__amdgpu_buffer_rsrc_t srd, unsigned voff, unsigned soff) {
  return __builtin_amdgcn_raw_buffer_load_b128(srd, voff, soff, 0);
}
DI bf16x8 pack8(f32x4 a, f32x4 b) {
  bf16x4 lo = __builtin_convertvector(a, bf16x4), hi = __builtin_convertvector(b, bf16x4);
  return cat(lo, hi);
}
DI f32x4 unpack4(bf16x4 v) {
  const u32x2 u = __builtin_bit_cast(u32x2, v);
  return f32x4{__uint_as_float(u[0] << 16), __uint_as_float(u[0] & 0xffff0000u), __uint_as_float(u[1] << 16), __uint_as_float(u[1] & 0xffff0000u)};
}
DI float max3f(float a, float b, float c) { float r; asm("v_max3_f32 %0, %1, %2, %3" : "=v"(r) : "v"(a), "v"(b), "v"(c)); return r; }
DI float max2f(float a, float b) { float r; asm("v_max_f32_e32 %0, %1, %2" : "=v"(r) : "v"(a), "v"(b)); return r; }
DI float xmax_g(float v) {
  auto a = __builtin_amdgcn_permlane16_swap(__float_as_uint(v), __float_as_uint(v), false, false);
  v = fmaxf(__uint_as_float(a[0]), __uint_as_float(a[1]));
  auto b = __builtin_amdgcn_permlane32_swap(__float_as_uint(v), __float_as_uint(v), false, false);
  return fmaxf(__uint_as_float(b[0]), __uint_as_float(b[1]));
}
DI float xsum_g(float v) {
  auto a = __builtin_amdgcn_permlane16_swap(__float_as_uint(v), __float_as_uint(v), false, false);
  v = __uint_as_float(a[0]) + __uint_as_float(a[1]);
  auto b = __builtin_amdgcn_permlane32_swap(__float_as_uint(v), __float_as_uint(v), false, false);
  return __uint_as_float(b[0]) + __uint_as_float(b[1]);
}
#define STAMP(i) do {} while (0)
#define STAMP_DUMP() do {} while (0)

__global__ __launch_bounds__(512, 2) void mha_kernel(const void* __restrict__ xg, const void* __restrict__ kg, const void* __restrict__ vg,
                                                     const void* __restrict__ wqg, const void* __restrict__ wog, void* __restrict__ outg,
                                                     void* __restrict__ ws) {
  extern __shared__ __attribute__((aligned(16))) char lds_raw[];
  const unsigned ldsb = (unsigned)(size_t)(AS3 char*)lds_raw;
  const int tid = threadIdx.x, lane = tid & 63;
  const int w = __builtin_amdgcn_readfirstlane(tid >> 6);
  const int r = lane & 15, g = lane >> 4;
  const int bid = blockIdx.x, xcd = bid & 7, idx = bid >> 3;
  const int b = idx >> 1, jj = 2 * xcd + (idx & 1), h0 = 2 * jj;
  const unsigned kvbase = (unsigned)(b * NH + h0) * (unsigned)(SK * DH * 2);
  const int lw = w & 3;
  STAMP(0);
  AS1 unsigned* const epw = (AS1 unsigned*)((char*)ws + WS_FLAGS) + (size_t)b * 17 * 32;
  const __amdgpu_buffer_rsrc_t srdP = __builtin_amdgcn_make_buffer_rsrc((char*)ws + WS_PART, 0, 256u * PART_BYTES, 0x00020000);
#define ABW W_BYTES
#define ABX X_BYTES
#define ABO W_BYTES
  const __amdgpu_buffer_rsrc_t srdW = __builtin_amdgcn_make_buffer_rsrc((void*)wqg, 0, ABW, 0x00020000);
  const __amdgpu_buffer_rsrc_t srdX = __builtin_amdgcn_make_buffer_rsrc((void*)xg, 0, ABX, 0x00020000);
  const __amdgpu_buffer_rsrc_t srdO = __builtin_amdgcn_make_buffer_rsrc((void*)wog, 0, ABO, 0x00020000);
  const unsigned xbase = (unsigned)b * (unsigned)(SQ * DM * 2);
  auto obuf_addr = [&](int c) -> unsigned { const int k = c % 5; return k < 2 ? WAREA + (unsigned)k * UNIT : (unsigned)(k - 2) * UNIT; };

  u32x4 cw = {0u, 0u, 0u, 0u};
  if (w == 4 || w == 5) {
    const char* pc0 = (const char*)__builtin_amdgcn_s_getpc();
    const unsigned off = (unsigned)idx * 2048u + (unsigned)(w - 4) * 1024u + lane * 16u;
    if (off + 16u <= CODE_WARM_BYTES) cw = *(const u32x4*)(pc0 + off);
  }
  if (w < 4) {
    const int rh = w & 1, kh = w >> 1;
    {
      unsigned voW[3], voX[3];
#pragma unroll
      for (int i = 0; i < 3; ++i) {
        const unsigned p = 64u * (3u * lw + i) + lane;
        const unsigned row = p / 24u, c = p % 24u;
        voW[i] = row * 12288u + ((c ^ ((((row >> 1) & 1u) | (((row >> 3) & 1u) << 1)) << 1)) << 4);
        const unsigned rx = p >> 3, cx = p & 7u;
        voX[i] = rx * 1280u + ((cx ^ ((rx >> 1) & 7u)) << 4);
      }
      const unsigned stw = ldsb + 3072u * lw + lane * 16u;
      const unsigned wq0 = (unsigned)h0 * 384u;
      constexpr int QD = 4;
      u32x4 R[QD][9];
      auto ldc = [&](int s, int c) {
        const unsigned wo = (unsigned)(64 * c) * 12288u + wq0;
#pragma unroll
        for (int i = 0; i < 3; ++i) R[s][i] = bload(srdW, voW[i], wo);
#pragma unroll
        for (int i = 0; i < 3; ++i) R[s][3 + i] = bload(srdW, voW[i], wo + 32u * 12288u);
#pragma unroll
        for (int i = 0; i < 3; ++i) R[s][6 + i] = bload(srdX, voX[i], xbase + (unsigned)c * 128u);
      };
      auto stc = [&](int s, int c) {
        const unsigned base = stw + ((c & 1) ? WAREA : QBUF);
#pragma unroll
        for (int j = 0; j < 3; ++j)
#pragma unroll
          for (int i = 0; i < 3; ++i) stu4(base + (unsigned)j * SLOT + 1024u * i, R[s][3 * j + i]);
      };
      ldc(0, 0); ldc(1, 1); ldc(2, 2);
      u32x4 wd[4], we[8];
      SB();
      STAMP(104);
      stc(0, 0);
      STAMP(105);
      ldc(3, 3); ldc(0, 4);
      bar();
      STAMP(106);
#pragma unroll
      for (int c = 0; c < 9; ++c) {
        stc((c + 1) % QD, c + 1);
        if (c + 1 + QD <= 9) ldc((c + 1) % QD, c + 1 + QD);
        if (c == WARMC) {
#pragma unroll
          for (int i = 0; i < 4; ++i) {
            const unsigned e = 60u * i + (lane < 60 ? lane : 59);
            const unsigned row = (unsigned)(40 * b + 10 * lw) + e / 24u;
            wd[i] = bload(srdW, row * 12288u + (e % 24u) * 16u, (unsigned)(h0 + 1) * 384u);
          }
          const unsigned so = (unsigned)(h0 * 192 + 24 * b) * 1280u + (unsigned)lw * 7680u;
#pragma unroll
          for (int i = 0; i < 8; ++i) we[i] = bload(srdO, (i < 7 || lane < 32 ? lane : 31) * 16u, so + 1024u * i);
        }
        STAMP(107 + c);
        bar();
      }
#pragma unroll
      for (int i = 0; i < 4; ++i) asm volatile("" :: "v"(wd[i]));
#pragma unroll
      for (int i = 0; i < 8; ++i) asm volatile("" :: "v"(we[i]));
    }
    STAMP(1);
    const unsigned ln = lane_id();
    const unsigned r2 = ln & 15u, g2 = ln >> 4;
    const unsigned fk = (r2 >> 1) & 7;
    const unsigned ka0 = ldsb + kh * SLOT + 384u * r2 + ((g2 ^ fk) << 4);
    const unsigned ka1 = ldsb + kh * SLOT + 384u * r2 + (((4u + g2) ^ fk) << 4);
    const unsigned qa = ldsb + QBUF + (unsigned)rh * 18432u + ln * 16u;
    const unsigned qq = r2 >> 2, pp = r2 & 3;
    const unsigned fv = (2u * (g2 & 1) + (qq >> 1)) << 1;
    unsigned va[4];
#pragma unroll
    for (int d = 0; d < 4; ++d) va[d] = ldsb + kh * SLOT + 384u * (4u * g2 + qq) + (((2u * d + (pp >> 1)) ^ fv) << 4) + 8u * (pp & 1);

    bf16x8 Of1[6][3];
#pragma unroll
    for (int pr = 0; pr < 2; ++pr) {
      f32x4 O[12][3];
#pragma unroll
      for (int d = 0; d < 12; ++d)
#pragma unroll
        for (int q = 0; q < 3; ++q) O[d][q] = f32x4{0.f, 0.f, 0.f, 0.f};
      float mrun[3] = {-INFINITY, -INFINITY, -INFINITY};
      f32x2 lsum2[3] = {f32x2{0.f, 0.f}, f32x2{0.f, 0.f}, f32x2{0.f, 0.f}};
      unsigned kpos = (unsigned)((32 * pr) % 3) * UNIT;
      for (int t = 0; t < 16; ++t) {
        const unsigned vpos = kpos == 2u * UNIT ? 0u : kpos + UNIT;
        f32x4 S[2][3];
        {
          bf16x8 kf[3][2], qf[3][3];
          auto ldq = [&](int s) {
#pragma unroll
            for (int q = 0; q < 3; ++q) qf[s % 3][q] = ld128(qa + (unsigned)(q * 6 + s) * 1024u);
          };
          const unsigned k0 = ka0 + kpos, k1 = ka1 + kpos;
          auto ldk = [&](int s) {
            const unsigned kb = ((s & 1) ? k1 : k0) + 128u * (s >> 1);
            kf[s % 3][0] = ld128(kb); kf[s % 3][1] = ld128(kb + 6144u);
          };
          if (t > 0) { ldq(0); ldq(1); }
          asm volatile("s_barrier" ::: "memory");
          STAMP(2 + 3 * (pr * 16 + t));
          if (t == 0) { ldq(0); ldq(1); }
          ldk(0); ldk(1);
#pragma unroll
          for (int q = 0; q < 3; ++q) { S[0][q] = f32x4{0.f, 0.f, 0.f, 0.f}; S[1][q] = f32x4{0.f, 0.f, 0.f, 0.f}; }
#pragma unroll
          for (int s = 0; s < 6; ++s) {
            SB();
            if (s + 2 < 6) { ldk(s + 2); ldq(s + 2); }
            SB();
#pragma unroll
            for (int q = 0; q < 3; ++q) {
              S[0][q] = mfma(kf[s % 3][0], qf[s % 3][q], S[0][q]);
              S[1][q] = mfma(kf[s % 3][1], qf[s % 3][q], S[1][q]);
            }
          }
          SB();
        }
        bar();
        STAMP(4 + 3 * (pr * 16 + t));
        bf16x8 vf[7];
        auto ldv = [&](int d) {
          const unsigned a = va[d & 3] + vpos + 128u * (d >> 2);
          vf[d % 7] = cat(ldtr(a), ldtr(a + 6144u));
        };
        ldv(0); ldv(1); ldv(2); ldv(3); ldv(4); ldv(5);
        SB();
        float mx[3];
        bool need = false;
#pragma unroll
        for (int q = 0; q < 3; ++q) {
          mx[q] = max3f(max3f(S[0][q][0], S[0][q][1], S[0][q][2]), max3f(S[0][q][3], S[1][q][0], S[1][q][1]), max2f(S[1][q][2], S[1][q][3]));
          need |= (mx[q] > mrun[q] + RESC_THR);
        }
        if (__any(need)) {
#pragma unroll
          for (int q = 0; q < 3; ++q) {
            const float mn = fmaxf(mrun[q], xmax_g(mx[q]));
            const float al = __builtin_amdgcn_exp2f((mrun[q] - mn) * SM_C);
            mrun[q] = mn;
            lsum2[q] *= al;
#pragma unroll
            for (int d = 0; d < 12; ++d) O[d][q] *= al;
          }
        }
        bf16x8 pf[3];
#pragma unroll
        for (int q = 0; q < 3; ++q) {
          const float nm = -mrun[q] * SM_C;
          const f32x2 nm2 = {nm, nm}, c2 = {SM_C, SM_C};
          f32x2 e[4];
#pragma unroll
          for (int i = 0; i < 4; ++i) {
            const f32x2 sv = {S[i >> 1][q][2 * (i & 1)], S[i >> 1][q][2 * (i & 1) + 1]};
            const f32x2 a = sv * c2 + nm2;
            e[i] = f32x2{__builtin_amdgcn_exp2f(a[0]), __builtin_amdgcn_exp2f(a[1])};
          }
          lsum2[q] += (e[0] + e[1]) + (e[2] + e[3]);
          pf[q] = pack8(f32x4{e[0][0], e[0][1], e[1][0], e[1][1]}, f32x4{e[2][0], e[2][1], e[3][0], e[3][1]});
        }
        STAMP(3 + 3 * (pr * 16 + t));
#pragma unroll
        for (int d = 0; d < 12; ++d) {
          SB();
          if (d + 6 < 12) ldv(d + 6);
          SB();
#pragma unroll
          for (int q = 0; q < 3; ++q) O[d][q] = mfma(vf[d % 7], pf[q], O[d][q]);
        }
        SB();
        asm volatile("s_waitcnt lgkmcnt(0)" ::: "memory");
        kpos = vpos == 2u * UNIT ? 0u : vpos + UNIT;
      }
      float lsum[3];
      const unsigned lm = lane_id(), rm = lm & 15u;
      {
        const unsigned sa = ldsb + MISC + (unsigned)w * 384u + rm * 8u;
#pragma unroll
        for (int q = 0; q < 3; ++q) {
          lsum[q] = xsum_g(lsum2[q][0] + lsum2[q][1]);
          if (lm < 16) *(AS3 f32x2*)(size_t)(sa + q * 128u) = f32x2{mrun[q], lsum[q]};
        }
      }
      bar();
      float sc[3];
      {
        const unsigned sb = ldsb + MISC + (unsigned)(w ^ 2) * 384u + rm * 8u;
#pragma unroll
        for (int q = 0; q < 3; ++q) {
          const f32x2 o = *(const AS3 f32x2*)(size_t)(sb + q * 128u);
          const float mn = fmaxf(mrun[q], o[0]);
          const float a0 = __builtin_amdgcn_exp2f((mrun[q] - mn) * SM_C), a1 = __builtin_amdgcn_exp2f((o[0] - mn) * SM_C);
          const float l = lsum[q] * a0 + o[1] * a1;
          sc[q] = a0 / l;
        }
      }
      const unsigned xreg = pr == 0 ? WAREA : 0u;
      const unsigned lx = lane_id() * 16u;
      const unsigned xw = ldsb + xreg + (unsigned)w * 18432u + lx;
      const unsigned xr = ldsb + xreg + (unsigned)(w ^ 2) * 18432u + lx;
      if (kh) {
        asm volatile("" ::: "memory");
#pragma unroll
        for (int d = 0; d < 6; ++d)
#pragma unroll
          for (int q = 0; q < 3; ++q) { stf4(xw + (unsigned)(d * 3 + q) * 1024u, O[d][q] * sc[q]); O[d][q] = O[6 + d][q] * sc[q]; }
        asm volatile("" ::: "memory");
      } else {
#pragma unroll
        for (int d = 0; d < 6; ++d)
#pragma unroll
          for (int q = 0; q < 3; ++q) { stf4(xw + (unsigned)(d * 3 + q) * 1024u, O[6 + d][q] * sc[q]); O[d][q] = O[d][q] * sc[q]; }
      }
      bar();
#pragma unroll
      for (int d = 0; d < 6; ++d)
#pragma unroll
        for (int q = 0; q < 3; ++q) O[d][q] += ldf4(xr + (unsigned)(d * 3 + q) * 1024u);
      asm volatile("s_waitcnt lgkmcnt(0)" ::: "memory");
#pragma unroll
      for (int q = 0; q < 3; ++q)
#pragma unroll
        for (int s = 0; s < 3; ++s) st128(xr + (unsigned)(q * 3 + s) * 1024u, pack8(O[2 * s][q], O[2 * s + 1][q]));
      bar();
      STAMP(100 + pr);
      if (pr == 1) {
#pragma unroll
        for (int k2 = 0; k2 < 2; ++k2) {
          const unsigned src = ldsb + (unsigned)(rh + 2 * (1 - k2)) * 18432u + lx;
#pragma unroll
          for (int q = 0; q < 3; ++q)
#pragma unroll
            for (int s2 = 0; s2 < 3; ++s2) Of1[3 * k2 + s2][q] = ld128(src + (unsigned)(q * 3 + s2) * 1024u);
        }
      }
      bar();
    }
    {
      const unsigned l4 = lane_id();
      const unsigned fo = ((l4 >> 4) & 3u) << 1;
      const unsigned voO = (l4 >> 3) * 1280u + (((l4 & 7u) ^ fo) << 4);
      const unsigned wob = (unsigned)(h0 + 1) * 192u * 1280u + (unsigned)lw * 48u * 1280u;
      auto issue = [&](int c) {
        const unsigned la = ldsb + obuf_addr(c) + (unsigned)lw * 6144u;
#pragma unroll
        for (int i = 0; i < 6; ++i) dma16(rfl(la + 1024u * i), voO, srdO, rfl(wob + (unsigned)i * 10240u + (unsigned)c * 128u));
      };
      issue(2); issue(3); issue(4);
      const unsigned g4 = l4 >> 4, qq4 = (l4 & 15u) >> 2, pp4 = l4 & 3u;
      const unsigned fv4 = (2u * (g4 & 1) + (qq4 >> 1)) << 1;
      const unsigned oa1 = ldsb + 128u * (4u * g4 + qq4) + (((4u * kh + 2u + (pp4 >> 1)) ^ fv4) << 4) + 8u * (pp4 & 1);
      const unsigned exw = ldsb + QBUF + (unsigned)lw * 3072u + l4 * 16u;
      bar();
      for (int c = 0; c < 10; ++c) {
        unsigned cb = oa1 + obuf_addr(c);
        asm volatile("" : "+v"(cb));
        bf16x8 af[6];
#pragma unroll
        for (int ks = 0; ks < 6; ++ks) af[ks] = cat(ldtr(cb + 4096u * ks), ldtr(cb + 4096u * ks + 2048u));
        SB();
        if (c >= 1 && c + 4 <= 9) issue(c + 4);
        SB();
        f32x4 acc[3] = {f32x4{0.f, 0.f, 0.f, 0.f}, f32x4{0.f, 0.f, 0.f, 0.f}, f32x4{0.f, 0.f, 0.f, 0.f}};
#pragma unroll
        for (int ks = 0; ks < 6; ++ks)
#pragma unroll
          for (int q = 0; q < 3; ++q) acc[q] = mfma(af[ks], Of1[ks][q], acc[q]);
#pragma unroll
        for (int q = 0; q < 3; ++q) stf4(exw + (unsigned)(c & 1) * 12288u + 1024u * q, acc[q]);
        if (c <= 5) waitvm<18>(); else if (c == 6) waitvm<12>(); else if (c == 7) waitvm<6>(); else waitvm<0>();
        bar();
      }
    }
  } else {
    const int hw = w - 4, rh = hw & 1, xh = hw >> 1;
    __builtin_amdgcn_s_setprio(1);
    const __amdgpu_buffer_rsrc_t srdK = __builtin_amdgcn_make_buffer_rsrc((void*)kg, 0, KV_BYTES, 0x00020000);
    const __amdgpu_buffer_rsrc_t srdV = __builtin_amdgcn_make_buffer_rsrc((void*)vg, 0, KV_BYTES, 0x00020000);
    unsigned fpk = 0;
#pragma unroll
    for (int i = 0; i < 3; ++i) {
      const unsigned row = (64u * i + lane) / 24u;
      fpk |= ((row >> 1) & 7u) << (3 * i);
      fpk |= (((row >> 1) & 3u) << 1) << (16 + 3 * i);
    }
    const unsigned ldsw = ldsb + 6144u * hw;
    auto issueKV = [&](int u) {
      const unsigned pos = ldsw + (unsigned)(u % 3) * UNIT;
      const unsigned go = kvbase + (unsigned)(u >> 1) * UNIT;
      const unsigned l = lane_id();
      const unsigned base = 128u * (l >> 3) + 6144u * hw, c7 = l & 7u;
      if (u & 1) {
#pragma unroll
        for (int i = 0; i < 3; ++i) {
          const unsigned vo = base + ((c7 ^ ((fpk >> (16 + 3 * i)) & 7u)) << 4);
          dma16nt(rfl(pos + 1024u * i), vo, srdV, rfl(go + 1024u * i));
          dma16nt(rfl(pos + 3072u + 1024u * i), vo, srdV, rfl(go + 3072u + 1024u * i));
        }
      } else {
#pragma unroll
        for (int i = 0; i < 3; ++i) {
          const unsigned vo = base + ((c7 ^ ((fpk >> (3 * i)) & 7u)) << 4);
          dma16nt(rfl(pos + 1024u * i), vo, srdK, rfl(go + 1024u * i));
          dma16nt(rfl(pos + 3072u + 1024u * i), vo ^ 64u, srdK, rfl(go + 3072u + 1024u * i));
        }
      }
    };
    {
      const unsigned dst = rfl(ldsb + 2u * UNIT + 1024u * hw);
      const unsigned so = (unsigned)idx * 61440u + (unsigned)hw * 15360u;
#pragma unroll
      for (int i = 0; i < 15; ++i) dma16(dst, lane * 16u, srdX, rfl(so + 1024u * i));
#pragma unroll
      for (int i = 0; i < 4; ++i) {
        const unsigned e = 60u * i + (lane < 60 ? lane : 59);
        const unsigned row = (unsigned)(40 * b + 10 * hw) + e / 24u;
        dma16(dst, row * 12288u + (e % 24u) * 16u, srdW, rfl((unsigned)h0 * 384u));
      }
    }
    const unsigned qq = r >> 2, pp = r & 3;
    const unsigned fw = ((qq >> 1) | (((unsigned)g & 1u) << 1)) << 1;
    unsigned wa[6];
#pragma unroll
    for (int d = 0; d < 6; ++d) {
      const unsigned dg = 6u * xh + d;
      wa[d] = 384u * (8u * g + qq) + (((2u * (dg & 3u) + (pp >> 1)) ^ fw) << 4) + 8u * (pp & 1) + 128u * (dg >> 2);
    }
    asm volatile("" :: "v"(cw));
    unsigned E = rfl(__hip_atomic_load(epw, __ATOMIC_RELAXED, __HIP_MEMORY_SCOPE_AGENT));
    asm volatile("" : "+s"(E));
    f32x4 QA[6][3];
    auto zeroQA = [&]() {
#pragma unroll
      for (int d = 0; d < 6; ++d)
#pragma unroll
        for (int q = 0; q < 3; ++q) QA[d][q] = f32x4{0.f, 0.f, 0.f, 0.f};
    };
    auto writeQ = [&]() {
      const unsigned l = lane_id(), rr = l & 15u, gg = l >> 4;
      const unsigned qw = ldsb + QBUF + (unsigned)rh * 18432u + (rr + 16u * (gg >> 1)) * 16u + 8u * (gg & 1u) + (unsigned)xh * 3072u;
#pragma unroll
      for (int q = 0; q < 3; ++q)
#pragma unroll
        for (int d = 0; d < 6; ++d)
          *(AS3 bf16x4*)(size_t)(qw + (unsigned)(q * 6 + (d >> 1)) * 1024u + 512u * (d & 1)) = __builtin_convertvector(QA[d][q], bf16x4);
    };
    auto qload = [&](unsigned wb, bf16x8 (&af)[6]) {
#pragma unroll
      for (int i = 0; i < 6; ++i) { const unsigned ad = wb + wa[i]; af[i] = cat(ldtr(ad), ldtr(ad + 1536u)); }
    };
    auto qmma = [&](const bf16x8 (&af)[6], const bf16x8 (&xf)[3]) {
#pragma unroll
      for (int i = 0; i < 6; ++i)
#pragma unroll
        for (int q = 0; q < 3; ++q) QA[i][q] = mfma(af[i], xf[q], QA[i][q]);
    };
    zeroQA();
    {
      const unsigned fk = (r >> 1) & 7;
      const unsigned xa0 = 128u * (48u * rh + r) + (((unsigned)g ^ fk) << 4);
      const unsigned xa1 = 128u * (48u * rh + r) + (((4u + g) ^ fk) << 4);
      for (int c = 0; c < 10; ++c) {
        bar();
        STAMP(98 + c);
        if (c == KV0) issueKV(0); else if (c == KV1) issueKV(1); else if (c == KV2) issueKV(2);
        const unsigned base = ldsb + ((c & 1) ? WAREA : QBUF);
        bf16x8 xf0[3], xf1[3], af0[6], af1[6];
#pragma unroll
        for (int q = 0; q < 3; ++q) xf0[q] = ld128(base + 2u * SLOT + xa0 + 2048u * q);
        qload(base, af0);
        SB();
#pragma unroll
        for (int q = 0; q < 3; ++q) xf1[q] = ld128(base + 2u * SLOT + xa1 + 2048u * q);
#pragma unroll
        for (int i = 0; i < 6; ++i) {
          SB();
          { const unsigned ad = base + SLOT + wa[i]; af1[i] = cat(ldtr(ad), ldtr(ad + 1536u)); }
          SB();
#pragma unroll
          for (int q = 0; q < 3; ++q) QA[i][q] = mfma(af0[i], xf0[q], QA[i][q]);
        }
        SB();
        qmma(af1, xf1);
        SB();
      }
    }
    writeQ();
    zeroQA();
    {
      unsigned voW[3], voX0, voX1;
#pragma unroll
      for (int i = 0; i < 3; ++i) {
        const unsigned p = 64u * (3u * hw + i) + lane;
        const unsigned row = p / 24u, c = p % 24u;
        voW[i] = row * 12288u + ((c ^ ((((row >> 1) & 1u) | (((row >> 3) & 1u) << 1)) << 1)) << 4);
      }
      {
        const unsigned p0 = 96u * hw + lane, p1 = p0 + 64u;
        voX0 = (p0 >> 2) * 1280u + (((p0 & 3u) ^ ((0u - (p0 >> 4)) & 3u)) << 4);
        voX1 = (p1 >> 2) * 1280u + (((p1 & 3u) ^ ((0u - (p1 >> 4)) & 3u)) << 4);
      }
      const unsigned xra = 64u * (48u * rh + r) + ((((unsigned)g) ^ ((0u - ((unsigned)r >> 2)) & 3u)) << 4);
      const unsigned wq1 = (unsigned)(h0 + 1) * 384u;
      int prev = 12;
      int jn = 0, jc = 0;
      for (int h = 0; h < 32; ++h) {
        wait_vm_dyn(prev);
        STAMP(1 + h);
        bar();
        int cnt = 0;
        const int hm = h % 3;
        const bool docomp = (h >= 2 && hm != 1 && jc < 20);
        bf16x8 xf[3], af[6];
        if (docomp) {
          const unsigned wbuf = ldsb + WAREA + (unsigned)(jc & 1) * 18432u;
#pragma unroll
          for (int q = 0; q < 3; ++q) xf[q] = ld128(wbuf + SLOT + xra + 1024u * q);
          qload(wbuf, af);
        }
        SB();
        if (h >= 1) { issueKV(h + 2); }
        cnt += 6;
        if (hm != 2 && jn < 20) {
          const unsigned wbuf = ldsb + WAREA + (unsigned)(jn & 1) * 18432u;
          const unsigned so = (unsigned)(32 * jn) * 12288u + wq1;
#pragma unroll
          for (int i = 0; i < 3; ++i) dma16(rfl(wbuf + 3072u * hw + 1024u * i), voW[i], srdW, rfl(so));
          const unsigned xo = xbase + (unsigned)jn * 64u;
          dma16(rfl(wbuf + SLOT + 1536u * hw), voX0, srdX, rfl(xo));
          if (lane < 32) dma16(rfl(wbuf + SLOT + 1536u * hw + 1024u), voX1, srdX, rfl(xo));
          ++jn; cnt += 5;
        }
        if (false && h == 26) {
          const unsigned so = (unsigned)(h0 * 192 + 24 * b) * 1280u + (unsigned)hw * 7680u;
#pragma unroll
          for (int i = 0; i < 7; ++i) dma16(rfl(ldsb + WAREA + 36864u + 3072u * hw), lane * 16u, srdO, rfl(so + 1024u * i));
          if (lane < 32) dma16(rfl(ldsb + WAREA + 36864u + 3072u * hw), lane * 16u, srdO, rfl(so + 7168u));
          cnt += 8;
        }
        SB();
        if (docomp) { qmma(af, xf); ++jc; }
        SB();
        prev = cnt;
      }
    }
    bar();
    issueKV(34);
    bar(); bar();
    bf16x8 Of[6][3];
    auto readO = [&](unsigned xreg) {
#pragma unroll
      for (int k2 = 0; k2 < 2; ++k2) {
        const unsigned src = ldsb + xreg + (unsigned)(rh + 2 * (1 - k2)) * 18432u + lane_id() * 16u;
#pragma unroll
        for (int q = 0; q < 3; ++q)
#pragma unroll
          for (int s = 0; s < 3; ++s) Of[3 * k2 + s][q] = ld128(src + (unsigned)(q * 3 + s) * 1024u);
      }
    };
    readO(WAREA);
    bar();
    writeQ();
    const unsigned l3 = lane_id(), g3 = l3 >> 4, qq3 = (l3 & 15u) >> 2, pp3 = l3 & 3u;
    const unsigned fv = (2u * (g3 & 1) + (qq3 >> 1)) << 1;
    const unsigned oa0 = 128u * (4u * g3 + qq3) + (((4u * xh + (pp3 >> 1)) ^ fv) << 4) + 8u * (pp3 & 1);
    const unsigned fo = ((l3 >> 4) & 3u) << 1;
    const unsigned voO = (l3 >> 3) * 1280u + (((l3 & 7u) ^ fo) << 4);
    u32x10 PP[2][3][2];
#pragma unroll
    for (int nb = 0; nb < 2; ++nb)
#pragma unroll
      for (int q = 0; q < 3; ++q)
#pragma unroll
        for (int d = 0; d < 2; ++d)
#pragma unroll
          for (int c = 0; c < 10; ++c) PP[nb][q][d][c] = 0u;
    {
      const unsigned wob0 = (unsigned)h0 * 192u * 1280u + (unsigned)hw * 48u * 1280u;
      auto half = [&](int h, int nb, int cc, bool comp, int ci, int prev) -> int {
        wait_vm_dyn(prev);
        STAMP(33 + h);
        bar();
        bf16x8 af[4];
        unsigned oab = (oa0 ^ (32u * nb)) + ldsb + WAREA + (unsigned)(cc & 1) * UNIT;
        asm volatile("" : "+v"(oab));
        auto ldo = [&](int ks) { af[ks & 3] = cat(ldtr(oab + 4096u * ks), ldtr(oab + 4096u * ks + 2048u)); };
        if (comp) { ldo(0); ldo(1); ldo(2); ldo(3); }
        SB();
        int cnt = 0;
        if (h >= 1 && 34 + h < 64) issueKV(34 + h);
        if (34 + h < 64) cnt += 6;
        if (ci >= 0) {
          const unsigned wob = wob0 + (ci >= 10 ? 192u * 1280u + (unsigned)(ci - 10) * 128u : (unsigned)ci * 128u);
#pragma unroll
          for (int i = 0; i < 6; ++i) dma16(rfl(ldsb + WAREA + (unsigned)(ci & 1) * UNIT + 6144u * hw + 1024u * i), voO, srdO, rfl(wob + (unsigned)i * 10240u));
          cnt += 6;
        }
        SB();
        if (comp) {
          f32x4 acc[3] = {f32x4{0.f, 0.f, 0.f, 0.f}, f32x4{0.f, 0.f, 0.f, 0.f}, f32x4{0.f, 0.f, 0.f, 0.f}};
#pragma unroll
          for (int ks = 0; ks < 6; ++ks) {
#pragma unroll
            for (int q = 0; q < 3; ++q) acc[q] = mfma(af[ks & 3], Of[ks][q], acc[q]);
            SB();
            if (ks + 4 < 6) ldo(ks + 4);
            SB();
          }
#pragma unroll
          for (int q = 0; q < 3; ++q) {
            const u32x2 pk = __builtin_bit_cast(u32x2, __builtin_convertvector(acc[q], bf16x4));
            PP[nb][q][0][cc] = pk[0]; PP[nb][q][1][cc] = pk[1];
          }
        }
        SB();
        STAMP(65 + h);
        return cnt;
      };
      int prev = 12;
      for (int i = 0; i < 16; ++i) {
        prev = half(2 * i, 1, i - 2, i >= 2 && i <= 11, -1, prev);
        prev = half(2 * i + 1, 0, i - 1, i >= 1 && i <= 10, i < 12 ? i : -1, prev);
      }
    }
    waitvm<0>();
    bar(); bar(); bar();
    readO(0u);
    bar();
    {
      const unsigned pbase = (unsigned)bid * PART_BYTES + (unsigned)hw * 30720u;
      const unsigned l16 = lane_id() * 16u;
      const unsigned exr = ldsb + QBUF + (unsigned)hw * 3072u + l16;
      bar();
      bf16x4 pk0[3];
      for (int c = 0; c <= 10; ++c) {
        if (c < 10) STAMP(108 + c);
        bf16x8 af[4];
        unsigned cb = oa0 + ldsb + obuf_addr(c);
        asm volatile("" : "+v"(cb));
        auto ldo = [&](int ks) { af[ks & 3] = cat(ldtr(cb + 4096u * ks), ldtr(cb + 4096u * ks + 2048u)); };
        if (c < 10) { ldo(0); ldo(1); ldo(2); ldo(3); }
        SB();
        if (c >= 1) {
          f32x4 ex[3];
#pragma unroll
          for (int q = 0; q < 3; ++q) ex[q] = ldf4(exr + (unsigned)((c - 1) & 1) * 12288u + 1024u * q);
#pragma unroll
          for (int q = 0; q < 3; ++q) {
            const unsigned u0 = PP[1][q][0][c - 1], u1 = PP[1][q][1][c - 1];
            const f32x4 p = f32x4{__uint_as_float(u0 << 16), __uint_as_float(u0 & 0xffff0000u), __uint_as_float(u1 << 16), __uint_as_float(u1 & 0xffff0000u)};
            __builtin_amdgcn_raw_buffer_store_b128(__builtin_bit_cast(u32x4, cat(pk0[q], __builtin_convertvector(ex[q] + p, bf16x4))), srdP, l16,
                                                   pbase + (unsigned)((c - 1) * 3 + q) * 1024u, 16  );
          }
        }
        SB();
        if (c < 10) {
          f32x4 acc[3];
#pragma unroll
          for (int q = 0; q < 3; ++q) {
            const unsigned u0 = PP[0][q][0][c], u1 = PP[0][q][1][c];
            acc[q] = f32x4{__uint_as_float(u0 << 16), __uint_as_float(u0 & 0xffff0000u), __uint_as_float(u1 << 16), __uint_as_float(u1 & 0xffff0000u)};
          }
#pragma unroll
          for (int ks = 0; ks < 6; ++ks) {
#pragma unroll
            for (int q = 0; q < 3; ++q) acc[q] = mfma(af[ks & 3], Of[ks][q], acc[q]);
            SB();
            if (ks + 4 < 6) ldo(ks + 4);
            SB();
          }
#pragma unroll
          for (int q = 0; q < 3; ++q) pk0[q] = __builtin_convertvector(acc[q], bf16x4);
          bar();
        }
      }
      waitvm<0>();
    }
    bar();
    const unsigned tag = E + 1u;
    if (hw == 0) {
      const unsigned l5 = lane_id();
      if (l5 == 0) __hip_atomic_store(epw + 32 * (1 + jj), tag, __ATOMIC_RELAXED, __HIP_MEMORY_SCOPE_AGENT);
      for (unsigned spin = 0; spin < (1u << 22); ++spin) {
        const unsigned v = __hip_atomic_load(epw + 32 * (1 + (l5 & 15)), __ATOMIC_RELAXED, __HIP_MEMORY_SCOPE_AGENT);
        if (__all(v == tag)) break;
        __builtin_amdgcn_s_sleep(1);
      }
      if (jj == 0 && l5 == 0) __hip_atomic_store(epw, tag, __ATOMIC_RELAXED, __HIP_MEMORY_SCOPE_AGENT);
      __builtin_amdgcn_fence(__ATOMIC_ACQUIRE, "wavefront");
    }
  }
  if (w < 4) bar();
  bar();
  STAMP(126);
  {
    const int piece = jj * 8 + w;
    const unsigned l6 = lane_id(), r6 = l6 & 15u, g6 = l6 >> 4;
    if (piece < 120) {
      float s[8];
#pragma unroll
      for (int i = 0; i < 8; ++i) s[i] = 0.f;
      u32x4 v[16];
#pragma unroll
      for (int k = 0; k < 16; ++k) {
        const unsigned obid = (unsigned)(((b * 2 + (k & 1)) << 3) | (k >> 1));
        v[k] = __builtin_amdgcn_raw_buffer_load_b128(srdP, l6 * 16u, obid * PART_BYTES + (unsigned)piece * 1024u, 16  );
      }
#pragma unroll
      for (int k = 0; k < 16; ++k)
#pragma unroll
        for (int i = 0; i < 4; ++i) { s[2 * i] += __uint_as_float(v[k][i] << 16); s[2 * i + 1] += __uint_as_float(v[k][i] & 0xffff0000u); }
      const int hp = piece / 30, rem = piece % 30, c = rem / 3, q = rem % 3;
      const int row = 48 * (hp & 1) + 16 * q + (int)r6, n0 = 64 * c + 32 * (hp >> 1) + 4 * (int)g6;
      __bf16* op = (__bf16*)outg + ((size_t)b * SQ + row) * DM + n0;
      *(bf16x4*)op = __builtin_convertvector((f32x4{s[0], s[1], s[2], s[3]}), bf16x4);
      *(bf16x4*)(op + 16) = __builtin_convertvector((f32x4{s[4], s[5], s[6], s[7]}), bf16x4);
    }
  }
  STAMP(127);
  STAMP_DUMP();
}

extern "C" void kernel_launch(void* const* d_in, const int* in_sizes, int n_in, void* d_out, int out_size, void* d_ws, size_t ws_size,
                              hipStream_t stream) {
  (void)in_sizes; (void)n_in; (void)out_size; (void)ws_size;
  (void)hipFuncSetAttribute((const void*)mha_kernel, hipFuncAttributeMaxDynamicSharedMemorySize, LDS_BYTES);
  mha_kernel<<<256, 512, LDS_BYTES, stream>>>(d_in[0], d_in[1], d_in[2], d_in[3], d_in[4], d_out, d_ws);
}
```

```cpp
#include <hip/hip_runtime.h>
#include <hip/hip_bf16.h>
#include <stdint.h>

typedef __bf16 bf16x8 __attribute__((ext_vector_type(8)));
typedef __bf16 bf16x4 __attribute__((ext_vector_type(4)));
typedef float f32x4 __attribute__((ext_vector_type(4)));
typedef float f32x2 __attribute__((ext_vector_type(2)));
typedef unsigned u32x4 __attribute__((ext_vector_type(4)));
typedef unsigned u32x2 __attribute__((ext_vector_type(2)));
typedef unsigned u32x10 __attribute__((ext_vector_type(10)));
#define AS3 __attribute__((address_space(3)))
#define AS1 __attribute__((address_space(1)))
#define DI __device__ __forceinline__
#define SB() __builtin_amdgcn_sched_barrier(0)

constexpr int NB = 16, SQ = 96, DM = 640, NH = 32, SK = 1024, DH = 192;
constexpr unsigned SLOT = 12288u, UNIT = 24576u;
constexpr unsigned WAREA = 3u * UNIT;
constexpr unsigned QBUF = 5u * UNIT;
constexpr unsigned MISC = QBUF + 36864u;
constexpr unsigned LDS_BYTES = 163840u;
constexpr unsigned KV_BYTES = (unsigned)NB * NH * SK * DH * 2u;
constexpr unsigned W_BYTES = (unsigned)DM * NH * DH * 2u;
constexpr unsigned X_BYTES = (unsigned)NB * SQ * DM * 2u;
constexpr unsigned PART_BYTES = 122880u;
constexpr unsigned WS_FLAGS = 0u;
constexpr unsigned WS_PART = 65536u;
#define WARMC 4
#define KV0 0
#define KV1 3
#define KV2 6
constexpr unsigned CODE_WARM_BYTES = 38912u;
constexpr float SM_C = 0.07216878364870322f * 1.4426950408889634f;
constexpr float RESC_THR = 6.0f / SM_C;

DI void dma16(unsigned ldsaddr, unsigned voff, __amdgpu_buffer_rsrc_t srd, unsigned soff) {
  asm volatile("s_mov_b32 m0, %0\n\ts_nop 0\n\tbuffer_load_dwordx4 %1, %2, %3 offen lds" :: "s"(ldsaddr), "v"(voff), "s"(srd), "s"(soff) : "memory", "m0");
}
DI void dma16nt(unsigned ldsaddr, unsigned voff, __amdgpu_buffer_rsrc_t srd, unsigned soff) {
  asm volatile("s_mov_b32 m0, %0\n\ts_nop 0\n\tbuffer_load_dwordx4 %1, %2, %3 offen nt lds" :: "s"(ldsaddr), "v"(voff), "s"(srd), "s"(soff) : "memory", "m0");
}
template <int N> DI void waitvm() { asm volatile("s_waitcnt vmcnt(%0)" :: "i"(N) : "memory"); }
DI void wait_vm_dyn(int n) {
  if (n >= 19) waitvm<19>(); else if (n >= 14) waitvm<14>(); else if (n >= 12) waitvm<12>(); else if (n == 11) waitvm<11>(); else if (n >= 6) waitvm<6>(); else if (n == 5) waitvm<5>(); else waitvm<0>();
}
DI void bar() { asm volatile("s_waitcnt lgkmcnt(0)\n\ts_barrier" ::: "memory"); }
DI unsigned lane_id() { unsigned l; asm volatile("v_mbcnt_lo_u32_b32 %0, -1, 0\n\tv_mbcnt_hi_u32_b32 %0, -1, %0" : "=v"(l)); return l; }
DI unsigned rfl(unsigned x) { return __builtin_amdgcn_readfirstlane(x); }
DI bf16x8 ld128(unsigned a) { return *(const AS3 bf16x8*)(size_t)a; }
DI f32x4 ldf4(unsigned a) { return *(const AS3 f32x4*)(size_t)a; }
DI void st128(unsigned a, bf16x8 v) { *(AS3 bf16x8*)(size_t)a = v; }
DI void stu4(unsigned a, u32x4 v) { *(AS3 u32x4*)(size_t)a = v; }
DI void stf4(unsigned a, f32x4 v) { *(AS3 f32x4*)(size_t)a = v; }
DI bf16x4 ldtr(unsigned a) { return __builtin_amdgcn_ds_read_tr16_b64_v4bf16((AS3 bf16x4*)(size_t)a); }
DI bf16x8 cat(bf16x4 lo, bf16x4 hi) { return __builtin_shufflevector(lo, hi, 0, 1, 2, 3, 4, 5, 6, 7); }
DI f32x4 mfma(bf16x8 a, bf16x8 b, f32x4 c) { return __builtin_amdgcn_mfma_f32_16x16x32_bf16(a, b, c, 0, 0, 0); }
DI u32x4 bload(__amdgpu_buffer_rsrc_t srd, unsigned voff, unsigned soff) {
  return __builtin_amdgcn_raw_buffer_load_b128(srd, voff, soff, 0);
}
DI bf16x8 pack8(f32x4 a, f32x4 b) {
  bf16x4 lo = __builtin_convertvector(a, bf16x4), hi = __builtin_convertvector(b, bf16x4);
  return cat(lo, hi);
}
DI f32x4 unpack4(bf16x4 v) {
  const u32x2 u = __builtin_bit_cast(u32x2, v);
  return f32x4{__uint_as_float(u[0] << 16), __uint_as_float(u[0] & 0xffff0000u), __uint_as_float(u[1] << 16), __uint_as_float(u[1] & 0xffff0000u)};
}
DI float max3f(float a, float b, float c) { float r; asm("v_max3_f32 %0, %1, %2, %3" : "=v"(r) : "v"(a), "v"(b), "v"(c)); return r; }
DI float max2f(float a, float b) { float r; asm("v_max_f32_e32 %0, %1, %2" : "=v"(r) : "v"(a), "v"(b)); return r; }
DI float xmax_g(float v) {
  auto a = __builtin_amdgcn_permlane16_swap(__float_as_uint(v), __float_as_uint(v), false, false);
  v = fmaxf(__uint_as_float(a[0]), __uint_as_float(a[1]));
  auto b = __builtin_amdgcn_permlane32_swap(__float_as_uint(v), __float_as_uint(v), false, false);
  return fmaxf(__uint_as_float(b[0]), __uint_as_float(b[1]));
}
DI float xsum_g(float v) {
  auto a = __builtin_amdgcn_permlane16_swap(__float_as_uint(v), __float_as_uint(v), false, false);
  v = __uint_as_float(a[0]) + __uint_as_float(a[1]);
  auto b = __builtin_amdgcn_permlane32_swap(__float_as_uint(v), __float_as_uint(v), false, false);
  return __uint_as_float(b[0]) + __uint_as_float(b[1]);
}
#define STAMP(i) do {} while (0)
#define STAMP_DUMP() do {} while (0)

__global__ __launch_bounds__(512, 2) void mha_kernel(const void* __restrict__ xg, const void* __restrict__ kg, const void* __restrict__ vg,
                                                     const void* __restrict__ wqg, const void* __restrict__ wog, void* __restrict__ outg,
                                                     void* __restrict__ ws) {
  extern __shared__ __attribute__((aligned(16))) char lds_raw[];
  const unsigned ldsb = (unsigned)(size_t)(AS3 char*)lds_raw;
  const int tid = threadIdx.x, lane = tid & 63;
  const int w = __builtin_amdgcn_readfirstlane(tid >> 6);
  const int r = lane & 15, g = lane >> 4;
  const int bid = blockIdx.x, xcd = bid & 7, idx = bid >> 3;
  const int b = idx >> 1, jj = 2 * xcd + (idx & 1), h0 = 2 * jj;
  const unsigned kvbase = (unsigned)(b * NH + h0) * (unsigned)(SK * DH * 2);
  const int lw = w & 3;
  STAMP(0);
  AS1 unsigned* const epw = (AS1 unsigned*)((char*)ws + WS_FLAGS) + (size_t)b * 17 * 32;
  const __amdgpu_buffer_rsrc_t srdP = __builtin_amdgcn_make_buffer_rsrc((char*)ws + WS_PART, 0, 256u * PART_BYTES, 0x00020000);
#define ABW W_BYTES
#define ABX X_BYTES
#define ABO W_BYTES
  const __amdgpu_buffer_rsrc_t srdW = __builtin_amdgcn_make_buffer_rsrc((void*)wqg, 0, ABW, 0x00020000);
  const __amdgpu_buffer_rsrc_t srdX = __builtin_amdgcn_make_buffer_rsrc((void*)xg, 0, ABX, 0x00020000);
  const __amdgpu_buffer_rsrc_t srdO = __builtin_amdgcn_make_buffer_rsrc((void*)wog, 0, ABO, 0x00020000);
  const unsigned xbase = (unsigned)b * (unsigned)(SQ * DM * 2);
  auto obuf_addr = [&](int c) -> unsigned { const int k = c % 6; return k < 2 ? WAREA + (unsigned)k * UNIT : (k < 5 ? (unsigned)(k - 2) * UNIT : QBUF); };

  u32x4 cw = {0u, 0u, 0u, 0u};
  if (w == 4 || w == 5) {
    const char* pc0 = (const char*)__builtin_amdgcn_s_getpc();
    const unsigned off = (unsigned)idx * 2048u + (unsigned)(w - 4) * 1024u + lane * 16u;
    if (off + 16u <= CODE_WARM_BYTES) cw = *(const u32x4*)(pc0 + off);
  }
  if (w < 4) {
    const int rh = w & 1, kh = w >> 1;
    {
      unsigned voW[3], voX[3];
#pragma unroll
      for (int i = 0; i < 3; ++i) {
        const unsigned p = 64u * (3u * lw + i) + lane;
        const unsigned row = p / 24u, c = p % 24u;
        voW[i] = row * 12288u + ((c ^ ((((row >> 1) & 1u) | (((row >> 3) & 1u) << 1)) << 1)) << 4);
        const unsigned rx = p >> 3, cx = p & 7u;
        voX[i] = rx * 1280u + ((cx ^ ((rx >> 1) & 7u)) << 4);
      }
      const unsigned stw = ldsb + 3072u * lw + lane * 16u;
      const unsigned wq0 = (unsigned)h0 * 384u;
      constexpr int QD = 4;
      u32x4 R[QD][9];
      auto ldc = [&](int s, int c) {
        const unsigned wo = (unsigned)(64 * c) * 12288u + wq0;
#pragma unroll
        for (int i = 0; i < 3; ++i) R[s][i] = bload(srdW, voW[i], wo);
#pragma unroll
        for (int i = 0; i < 3; ++i) R[s][3 + i] = bload(srdW, voW[i], wo + 32u * 12288u);
#pragma unroll
        for (int i = 0; i < 3; ++i) R[s][6 + i] = bload(srdX, voX[i], xbase + (unsigned)c * 128u);
      };
      auto stc = [&](int s, int c) {
        const unsigned base = stw + ((c & 1) ? WAREA : QBUF);
#pragma unroll
        for (int j = 0; j < 3; ++j)
#pragma unroll
          for (int i = 0; i < 3; ++i) stu4(base + (unsigned)j * SLOT + 1024u * i, R[s][3 * j + i]);
      };
      ldc(0, 0); ldc(1, 1); ldc(2, 2);
      u32x4 wd[4], we[8];
      SB();
      STAMP(104);
      stc(0, 0);
      STAMP(105);
      ldc(3, 3); ldc(0, 4);
      bar();
      STAMP(106);
#pragma unroll
      for (int c = 0; c < 9; ++c) {
        stc((c + 1) % QD, c + 1);
        if (c + 1 + QD <= 9) ldc((c + 1) % QD, c + 1 + QD);
        if (c == WARMC) {
#pragma unroll
          for (int i = 0; i < 4; ++i) {
            const unsigned e = 60u * i + (lane < 60 ? lane : 59);
            const unsigned row = (unsigned)(40 * b + 10 * lw) + e / 24u;
            wd[i] = bload(srdW, row * 12288u + (e % 24u) * 16u, (unsigned)(h0 + 1) * 384u);
          }
          const unsigned so = (unsigned)(h0 * 192 + 24 * b) * 1280u + (unsigned)lw * 7680u;
#pragma unroll
          for (int i = 0; i < 8; ++i) we[i] = bload(srdO, (i < 7 || lane < 32 ? lane : 31) * 16u, so + 1024u * i);
        }
        STAMP(107 + c);
        bar();
      }
#pragma unroll
      for (int i = 0; i < 4; ++i) asm volatile("" :: "v"(wd[i]));
#pragma unroll
      for (int i = 0; i < 8; ++i) asm volatile("" :: "v"(we[i]));
    }
    STAMP(1);
    const unsigned ln = lane_id();
    const unsigned r2 = ln & 15u, g2 = ln >> 4;
    const unsigned fk = (r2 >> 1) & 7;
    const unsigned ka0 = ldsb + kh * SLOT + 384u * r2 + ((g2 ^ fk) << 4);
    const unsigned ka1 = ldsb + kh * SLOT + 384u * r2 + (((4u + g2) ^ fk) << 4);
    const unsigned qa = ldsb + QBUF + (unsigned)rh * 18432u + ln * 16u;
    const unsigned qq = r2 >> 2, pp = r2 & 3;
    const unsigned fv = (2u * (g2 & 1) + (qq >> 1)) << 1;
    unsigned va[4];
#pragma unroll
    for (int d = 0; d < 4; ++d) va[d] = ldsb + kh * SLOT + 384u * (4u * g2 + qq) + (((2u * d + (pp >> 1)) ^ fv) << 4) + 8u * (pp & 1);

#pragma unroll
    for (int pr = 0; pr < 2; ++pr) {
      f32x4 O[12][3];
#pragma unroll
      for (int d = 0; d < 12; ++d)
#pragma unroll
        for (int q = 0; q < 3; ++q) O[d][q] = f32x4{0.f, 0.f, 0.f, 0.f};
      float mrun[3] = {-INFINITY, -INFINITY, -INFINITY};
      f32x2 lsum2[3] = {f32x2{0.f, 0.f}, f32x2{0.f, 0.f}, f32x2{0.f, 0.f}};
      unsigned kpos = (unsigned)((32 * pr) % 3) * UNIT;
      for (int t = 0; t < 16; ++t) {
        const unsigned vpos = kpos == 2u * UNIT ? 0u : kpos + UNIT;
        f32x4 S[2][3];
        {
          bf16x8 kf[3][2], qf[3][3];
          auto ldq = [&](int s) {
#pragma unroll
            for (int q = 0; q < 3; ++q) qf[s % 3][q] = ld128(qa + (unsigned)(q * 6 + s) * 1024u);
          };
          const unsigned k0 = ka0 + kpos, k1 = ka1 + kpos;
          auto ldk = [&](int s) {
            const unsigned kb = ((s & 1) ? k1 : k0) + 128u * (s >> 1);
            kf[s % 3][0] = ld128(kb); kf[s % 3][1] = ld128(kb + 6144u);
          };
          if (t > 0) { ldq(0); ldq(1); }
          asm volatile("s_barrier" ::: "memory");
          STAMP(2 + 3 * (pr * 16 + t));
          if (t == 0) { ldq(0); ldq(1); }
          ldk(0); ldk(1);
#pragma unroll
          for (int q = 0; q < 3; ++q) { S[0][q] = f32x4{0.f, 0.f, 0.f, 0.f}; S[1][q] = f32x4{0.f, 0.f, 0.f, 0.f}; }
#pragma unroll
          for (int s = 0; s < 6; ++s) {
            SB();
            if (s + 2 < 6) { ldk(s + 2); ldq(s + 2); }
            SB();
#pragma unroll
            for (int q = 0; q < 3; ++q) {
              S[0][q] = mfma(kf[s % 3][0], qf[s % 3][q], S[0][q]);
              S[1][q] = mfma(kf[s % 3][1], qf[s % 3][q], S[1][q]);
            }
          }
          SB();
        }
        bar();
        STAMP(4 + 3 * (pr * 16 + t));
        bf16x8 vf[7];
        auto ldv = [&](int d) {
          const unsigned a = va[d & 3] + vpos + 128u * (d >> 2);
          vf[d % 7] = cat(ldtr(a), ldtr(a + 6144u));
        };
        ldv(0); ldv(1); ldv(2); ldv(3); ldv(4); ldv(5);
        SB();
        float mx[3];
        bool need = false;
#pragma unroll
        for (int q = 0; q < 3; ++q) {
          mx[q] = max3f(max3f(S[0][q][0], S[0][q][1], S[0][q][2]), max3f(S[0][q][3], S[1][q][0], S[1][q][1]), max2f(S[1][q][2], S[1][q][3]));
          need |= (mx[q] > mrun[q] + RESC_THR);
        }
        if (__any(need)) {
#pragma unroll
          for (int q = 0; q < 3; ++q) {
            const float mn = fmaxf(mrun[q], xmax_g(mx[q]));
            const float al = __builtin_amdgcn_exp2f((mrun[q] - mn) * SM_C);
            mrun[q] = mn;
            lsum2[q] *= al;
#pragma unroll
            for (int d = 0; d < 12; ++d) O[d][q] *= al;
          }
        }
        bf16x8 pf[3];
#pragma unroll
        for (int q = 0; q < 3; ++q) {
          const float nm = -mrun[q] * SM_C;
          const f32x2 nm2 = {nm, nm}, c2 = {SM_C, SM_C};
          f32x2 e[4];
#pragma unroll
          for (int i = 0; i < 4; ++i) {
            const f32x2 sv = {S[i >> 1][q][2 * (i & 1)], S[i >> 1][q][2 * (i & 1) + 1]};
            const f32x2 a = sv * c2 + nm2;
            e[i] = f32x2{__builtin_amdgcn_exp2f(a[0]), __builtin_amdgcn_exp2f(a[1])};
          }
          lsum2[q] += (e[0] + e[1]) + (e[2] + e[3]);
          pf[q] = pack8(f32x4{e[0][0], e[0][1], e[1][0], e[1][1]}, f32x4{e[2][0], e[2][1], e[3][0], e[3][1]});
        }
        STAMP(3 + 3 * (pr * 16 + t));
#pragma unroll
        for (int d = 0; d < 12; ++d) {
          SB();
          if (d + 6 < 12) ldv(d + 6);
          SB();
#pragma unroll
          for (int q = 0; q < 3; ++q) O[d][q] = mfma(vf[d % 7], pf[q], O[d][q]);
        }
        SB();
        asm volatile("s_waitcnt lgkmcnt(0)" ::: "memory");
        kpos = vpos == 2u * UNIT ? 0u : vpos + UNIT;
      }
      float lsum[3];
      const unsigned lm = lane_id(), rm = lm & 15u;
      {
        const unsigned sa = ldsb + MISC + (unsigned)w * 384u + rm * 8u;
#pragma unroll
        for (int q = 0; q < 3; ++q) {
          lsum[q] = xsum_g(lsum2[q][0] + lsum2[q][1]);
          if (lm < 16) *(AS3 f32x2*)(size_t)(sa + q * 128u) = f32x2{mrun[q], lsum[q]};
        }
      }
      bar();
      float sc[3];
      {
        const unsigned sb = ldsb + MISC + (unsigned)(w ^ 2) * 384u + rm * 8u;
#pragma unroll
        for (int q = 0; q < 3; ++q) {
          const f32x2 o = *(const AS3 f32x2*)(size_t)(sb + q * 128u);
          const float mn = fmaxf(mrun[q], o[0]);
          const float a0 = __builtin_amdgcn_exp2f((mrun[q] - mn) * SM_C), a1 = __builtin_amdgcn_exp2f((o[0] - mn) * SM_C);
          const float l = lsum[q] * a0 + o[1] * a1;
          sc[q] = a0 / l;
        }
      }
      const unsigned xreg = pr == 0 ? WAREA : 0u;
      const unsigned lx = lane_id() * 16u;
      const unsigned xw = ldsb + xreg + (unsigned)w * 18432u + lx;
      const unsigned xr = ldsb + xreg + (unsigned)(w ^ 2) * 18432u + lx;
      if (kh) {
        asm volatile("" ::: "memory");
#pragma unroll
        for (int d = 0; d < 6; ++d)
#pragma unroll
          for (int q = 0; q < 3; ++q) { stf4(xw + (unsigned)(d * 3 + q) * 1024u, O[d][q] * sc[q]); O[d][q] = O[6 + d][q] * sc[q]; }
        asm volatile("" ::: "memory");
      } else {
#pragma unroll
        for (int d = 0; d < 6; ++d)
#pragma unroll
          for (int q = 0; q < 3; ++q) { stf4(xw + (unsigned)(d * 3 + q) * 1024u, O[6 + d][q] * sc[q]); O[d][q] = O[d][q] * sc[q]; }
      }
      bar();
#pragma unroll
      for (int d = 0; d < 6; ++d)
#pragma unroll
        for (int q = 0; q < 3; ++q) O[d][q] += ldf4(xr + (unsigned)(d * 3 + q) * 1024u);
      asm volatile("s_waitcnt lgkmcnt(0)" ::: "memory");
#pragma unroll
      for (int q = 0; q < 3; ++q)
#pragma unroll
        for (int s = 0; s < 3; ++s) st128(xr + (unsigned)(q * 3 + s) * 1024u, pack8(O[2 * s][q], O[2 * s + 1][q]));
      bar();
      STAMP(100 + pr);
      bar();
    }
    {
      const unsigned l4 = lane_id();
      const unsigned fo = ((l4 >> 4) & 3u) << 1;
      const unsigned voO = (l4 >> 3) * 1280u + (((l4 & 7u) ^ fo) << 4);
      const unsigned wob = (unsigned)(h0 + 1) * 192u * 1280u + (unsigned)lw * 48u * 1280u;
      auto issue = [&](int c) {
        const unsigned la = ldsb + obuf_addr(c) + (unsigned)lw * 6144u;
#pragma unroll
        for (int i = 0; i < 6; ++i) dma16(rfl(la + 1024u * i), voO, srdO, rfl(wob + (unsigned)i * 10240u + (unsigned)c * 128u));
      };
      issue(2); issue(3); issue(4); issue(5);
      for (int c = 0; c < 10; ++c) {
        if (c >= 1) {
          if (c <= 5) waitvm<18>(); else if (c == 6) waitvm<12>(); else if (c == 7) waitvm<6>(); else waitvm<0>();
        }
        bar();
        if (c >= 1 && c + 5 <= 9) issue(c + 5);
      }
    }
  } else {
    const int hw = w - 4, rh = hw & 1, xh = hw >> 1;
    __builtin_amdgcn_s_setprio(1);
    const __amdgpu_buffer_rsrc_t srdK = __builtin_amdgcn_make_buffer_rsrc((void*)kg, 0, KV_BYTES, 0x00020000);
    const __amdgpu_buffer_rsrc_t srdV = __builtin_amdgcn_make_buffer_rsrc((void*)vg, 0, KV_BYTES, 0x00020000);
    unsigned fpk = 0;
#pragma unroll
    for (int i = 0; i < 3; ++i) {
      const unsigned row = (64u * i + lane) / 24u;
      fpk |= ((row >> 1) & 7u) << (3 * i);
      fpk |= (((row >> 1) & 3u) << 1) << (16 + 3 * i);
    }
    const unsigned ldsw = ldsb + 6144u * hw;
    auto issueKV = [&](int u) {
      const unsigned pos = ldsw + (unsigned)(u % 3) * UNIT;
      const unsigned go = kvbase + (unsigned)(u >> 1) * UNIT;
      const unsigned l = lane_id();
      const unsigned base = 128u * (l >> 3) + 6144u * hw, c7 = l & 7u;
      if (u & 1) {
#pragma unroll
        for (int i = 0; i < 3; ++i) {
          const unsigned vo = base + ((c7 ^ ((fpk >> (16 + 3 * i)) & 7u)) << 4);
          dma16nt(rfl(pos + 1024u * i), vo, srdV, rfl(go + 1024u * i));
          dma16nt(rfl(pos + 3072u + 1024u * i), vo, srdV, rfl(go + 3072u + 1024u * i));
        }
      } else {
#pragma unroll
        for (int i = 0; i < 3; ++i) {
          const unsigned vo = base + ((c7 ^ ((fpk >> (3 * i)) & 7u)) << 4);
          dma16nt(rfl(pos + 1024u * i), vo, srdK, rfl(go + 1024u * i));
          dma16nt(rfl(pos + 3072u + 1024u * i), vo ^ 64u, srdK, rfl(go + 3072u + 1024u * i));
        }
      }
    };
    {
      const unsigned dst = rfl(ldsb + 2u * UNIT + 1024u * hw);
      const unsigned so = (unsigned)idx * 61440u + (unsigned)hw * 15360u;
#pragma unroll
      for (int i = 0; i < 15; ++i) dma16(dst, lane * 16u, srdX, rfl(so + 1024u * i));
#pragma unroll
      for (int i = 0; i < 4; ++i) {
        const unsigned e = 60u * i + (lane < 60 ? lane : 59);
        const unsigned row = (unsigned)(40 * b + 10 * hw) + e / 24u;
        dma16(dst, row * 12288u + (e % 24u) * 16u, srdW, rfl((unsigned)h0 * 384u));
      }
    }
    const unsigned qq = r >> 2, pp = r & 3;
    const unsigned fw = ((qq >> 1) | (((unsigned)g & 1u) << 1)) << 1;
    unsigned wa[6];
#pragma unroll
    for (int d = 0; d < 6; ++d) {
      const unsigned dg = 6u * xh + d;
      wa[d] = 384u * (8u * g + qq) + (((2u * (dg & 3u) + (pp >> 1)) ^ fw) << 4) + 8u * (pp & 1) + 128u * (dg >> 2);
    }
    asm volatile("" :: "v"(cw));
    unsigned E = rfl(__hip_atomic_load(epw, __ATOMIC_RELAXED, __HIP_MEMORY_SCOPE_AGENT));
    asm volatile("" : "+s"(E));
    f32x4 QA[6][3];
    auto zeroQA = [&]() {
#pragma unroll
      for (int d = 0; d < 6; ++d)
#pragma unroll
        for (int q = 0; q < 3; ++q) QA[d][q] = f32x4{0.f, 0.f, 0.f, 0.f};
    };
    auto writeQ = [&]() {
      const unsigned l = lane_id(), rr = l & 15u, gg = l >> 4;
      const unsigned qw = ldsb + QBUF + (unsigned)rh * 18432u + (rr + 16u * (gg >> 1)) * 16u + 8u * (gg & 1u) + (unsigned)xh * 3072u;
#pragma unroll
      for (int q = 0; q < 3; ++q)
#pragma unroll
        for (int d = 0; d < 6; ++d)
          *(AS3 bf16x4*)(size_t)(qw + (unsigned)(q * 6 + (d >> 1)) * 1024u + 512u * (d & 1)) = __builtin_convertvector(QA[d][q], bf16x4);
    };
    auto qload = [&](unsigned wb, bf16x8 (&af)[6]) {
#pragma unroll
      for (int i = 0; i < 6; ++i) { const unsigned ad = wb + wa[i]; af[i] = cat(ldtr(ad), ldtr(ad + 1536u)); }
    };
    auto qmma = [&](const bf16x8 (&af)[6], const bf16x8 (&xf)[3]) {
#pragma unroll
      for (int i = 0; i < 6; ++i)
#pragma unroll
        for (int q = 0; q < 3; ++q) QA[i][q] = mfma(af[i], xf[q], QA[i][q]);
    };
    zeroQA();
    {
      const unsigned fk = (r >> 1) & 7;
      const unsigned xa0 = 128u * (48u * rh + r) + (((unsigned)g ^ fk) << 4);
      const unsigned xa1 = 128u * (48u * rh + r) + (((4u + g) ^ fk) << 4);
      for (int c = 0; c < 10; ++c) {
        bar();
        STAMP(98 + c);
        if (c == KV0) issueKV(0); else if (c == KV1) issueKV(1); else if (c == KV2) issueKV(2);
        const unsigned base = ldsb + ((c & 1) ? WAREA : QBUF);
        bf16x8 xf0[3], xf1[3], af0[6], af1[6];
#pragma unroll
        for (int q = 0; q < 3; ++q) xf0[q] = ld128(base + 2u * SLOT + xa0 + 2048u * q);
        qload(base, af0);
        SB();
#pragma unroll
        for (int q = 0; q < 3; ++q) xf1[q] = ld128(base + 2u * SLOT + xa1 + 2048u * q);
#pragma unroll
        for (int i = 0; i < 6; ++i) {
          SB();
          { const unsigned ad = base + SLOT + wa[i]; af1[i] = cat(ldtr(ad), ldtr(ad + 1536u)); }
          SB();
#pragma unroll
          for (int q = 0; q < 3; ++q) QA[i][q] = mfma(af0[i], xf0[q], QA[i][q]);
        }
        SB();
        qmma(af1, xf1);
        SB();
      }
    }
    writeQ();
    zeroQA();
    {
      unsigned voW[3], voX0, voX1;
#pragma unroll
      for (int i = 0; i < 3; ++i) {
        const unsigned p = 64u * (3u * hw + i) + lane;
        const unsigned row = p / 24u, c = p % 24u;
        voW[i] = row * 12288u + ((c ^ ((((row >> 1) & 1u) | (((row >> 3) & 1u) << 1)) << 1)) << 4);
      }
      {
        const unsigned p0 = 96u * hw + lane, p1 = p0 + 64u;
        voX0 = (p0 >> 2) * 1280u + (((p0 & 3u) ^ ((0u - (p0 >> 4)) & 3u)) << 4);
        voX1 = (p1 >> 2) * 1280u + (((p1 & 3u) ^ ((0u - (p1 >> 4)) & 3u)) << 4);
      }
      const unsigned xra = 64u * (48u * rh + r) + ((((unsigned)g) ^ ((0u - ((unsigned)r >> 2)) & 3u)) << 4);
      const unsigned wq1 = (unsigned)(h0 + 1) * 384u;
      int prev = 12;
      int jn = 0, jc = 0;
      for (int h = 0; h < 32; ++h) {
        wait_vm_dyn(prev);
        STAMP(1 + h);
        bar();
        int cnt = 0;
        const int hm = h % 3;
        const bool docomp = (h >= 2 && hm != 1 && jc < 20);
        bf16x8 xf[3], af[6];
        if (docomp) {
          const unsigned wbuf = ldsb + WAREA + (unsigned)(jc & 1) * 18432u;
#pragma unroll
          for (int q = 0; q < 3; ++q) xf[q] = ld128(wbuf + SLOT + xra + 1024u * q);
          qload(wbuf, af);
        }
        SB();
        if (h >= 1) { issueKV(h + 2); }
        cnt += 6;
        if (hm != 2 && jn < 20) {
          const unsigned wbuf = ldsb + WAREA + (unsigned)(jn & 1) * 18432u;
          const unsigned so = (unsigned)(32 * jn) * 12288u + wq1;
#pragma unroll
          for (int i = 0; i < 3; ++i) dma16(rfl(wbuf + 3072u * hw + 1024u * i), voW[i], srdW, rfl(so));
          const unsigned xo = xbase + (unsigned)jn * 64u;
          dma16(rfl(wbuf + SLOT + 1536u * hw), voX0, srdX, rfl(xo));
          if (lane < 32) dma16(rfl(wbuf + SLOT + 1536u * hw + 1024u), voX1, srdX, rfl(xo));
          ++jn; cnt += 5;
        }
        if (false && h == 26) {
          const unsigned so = (unsigned)(h0 * 192 + 24 * b) * 1280u + (unsigned)hw * 7680u;
#pragma unroll
          for (int i = 0; i < 7; ++i) dma16(rfl(ldsb + WAREA + 36864u + 3072u * hw), lane * 16u, srdO, rfl(so + 1024u * i));
          if (lane < 32) dma16(rfl(ldsb + WAREA + 36864u + 3072u * hw), lane * 16u, srdO, rfl(so + 7168u));
          cnt += 8;
        }
        SB();
        if (docomp) { qmma(af, xf); ++jc; }
        SB();
        prev = cnt;
      }
    }
    bar();
    issueKV(34);
    bar(); bar();
    bf16x8 Of[6][3];
    auto readO = [&](unsigned xreg) {
#pragma unroll
      for (int k2 = 0; k2 < 2; ++k2) {
        const unsigned src = ldsb + xreg + (unsigned)(rh + 2 * (1 - k2)) * 18432u + lane_id() * 16u;
#pragma unroll
        for (int q = 0; q < 3; ++q)
#pragma unroll
          for (int s = 0; s < 3; ++s) Of[3 * k2 + s][q] = ld128(src + (unsigned)(q * 3 + s) * 1024u);
      }
    };
    readO(WAREA);
    bar();
    writeQ();
    const unsigned l3 = lane_id(), g3 = l3 >> 4, qq3 = (l3 & 15u) >> 2, pp3 = l3 & 3u;
    const unsigned fv = (2u * (g3 & 1) + (qq3 >> 1)) << 1;
    const unsigned oa0 = 128u * (4u * g3 + qq3) + (((4u * xh + (pp3 >> 1)) ^ fv) << 4) + 8u * (pp3 & 1);
    const unsigned fo = ((l3 >> 4) & 3u) << 1;
    const unsigned voO = (l3 >> 3) * 1280u + (((l3 & 7u) ^ fo) << 4);
    u32x10 PP[2][3][2];
#pragma unroll
    for (int nb = 0; nb < 2; ++nb)
#pragma unroll
      for (int q = 0; q < 3; ++q)
#pragma unroll
        for (int d = 0; d < 2; ++d)
#pragma unroll
          for (int c = 0; c < 10; ++c) PP[nb][q][d][c] = 0u;
    {
      const unsigned wob0 = (unsigned)h0 * 192u * 1280u + (unsigned)hw * 48u * 1280u;
      auto half = [&](int h, int nb, int cc, bool comp, int ci, int prev) -> int {
        wait_vm_dyn(prev);
        STAMP(33 + h);
        bar();
        bf16x8 af[4];
        unsigned oab = (oa0 ^ (32u * nb)) + ldsb + WAREA + (unsigned)(cc & 1) * UNIT;
        asm volatile("" : "+v"(oab));
        auto ldo = [&](int ks) { af[ks & 3] = cat(ldtr(oab + 4096u * ks), ldtr(oab + 4096u * ks + 2048u)); };
        if (comp) { ldo(0); ldo(1); ldo(2); ldo(3); }
        SB();
        int cnt = 0;
        if (h >= 1 && 34 + h < 64) issueKV(34 + h);
        if (34 + h < 64) cnt += 6;
        if (ci >= 0) {
          const unsigned wob = wob0 + (ci >= 10 ? 192u * 1280u + (unsigned)(ci - 10) * 128u : (unsigned)ci * 128u);
#pragma unroll
          for (int i = 0; i < 6; ++i) dma16(rfl(ldsb + WAREA + (unsigned)(ci & 1) * UNIT + 6144u * hw + 1024u * i), voO, srdO, rfl(wob + (unsigned)i * 10240u));
          cnt += 6;
        }
        SB();
        if (comp) {
          f32x4 acc[3] = {f32x4{0.f, 0.f, 0.f, 0.f}, f32x4{0.f, 0.f, 0.f, 0.f}, f32x4{0.f, 0.f, 0.f, 0.f}};
#pragma unroll
          for (int ks = 0; ks < 6; ++ks) {
#pragma unroll
            for (int q = 0; q < 3; ++q) acc[q] = mfma(af[ks & 3], Of[ks][q], acc[q]);
            SB();
            if (ks + 4 < 6) ldo(ks + 4);
            SB();
          }
#pragma unroll
          for (int q = 0; q < 3; ++q) {
            const u32x2 pk = __builtin_bit_cast(u32x2, __builtin_convertvector(acc[q], bf16x4));
            PP[nb][q][0][cc] = pk[0]; PP[nb][q][1][cc] = pk[1];
          }
        }
        SB();
        STAMP(65 + h);
        return cnt;
      };
      int prev = 12;
      for (int i = 0; i < 16; ++i) {
        prev = half(2 * i, 1, i - 2, i >= 2 && i <= 11, -1, prev);
        prev = half(2 * i + 1, 0, i - 1, i >= 1 && i <= 10, i < 12 ? i : -1, prev);
      }
    }
    waitvm<0>();
    bar(); bar(); bar();
    readO(0u);
    bar();
    {
      const unsigned pbase = (unsigned)bid * PART_BYTES + (unsigned)hw * 30720u;
      const unsigned l16 = lane_id() * 16u;
      bf16x8 af[4];
      bar();
      auto ldstep = [&](unsigned b0, unsigned b1, int st) {
        const unsigned ad = (st / 6 ? b1 : b0) + 4096u * (st % 6);
        af[st % 4] = cat(ldtr(ad), ldtr(ad + 2048u));
      };
      unsigned cb0 = oa0 + ldsb + obuf_addr(0), cb1 = (oa0 ^ 32u) + ldsb + obuf_addr(0);
      asm volatile("" : "+v"(cb0), "+v"(cb1));
#pragma unroll
      for (int st = 0; st < 3; ++st) ldstep(cb0, cb1, st);
      for (int c = 0; c < 10; ++c) {
        STAMP(108 + c);
        unsigned cn0 = oa0 + ldsb + obuf_addr(c + 1);
        asm volatile("" : "+v"(cn0));
        bf16x4 pk[2][3];
#pragma unroll
        for (int nb = 0; nb < 2; ++nb) {
          f32x4 acc[3];
#pragma unroll
          for (int q = 0; q < 3; ++q) {
            const unsigned u0 = PP[nb][q][0][c], u1 = PP[nb][q][1][c];
            acc[q] = f32x4{__uint_as_float(u0 << 16), __uint_as_float(u0 & 0xffff0000u), __uint_as_float(u1 << 16), __uint_as_float(u1 & 0xffff0000u)};
          }
#pragma unroll
          for (int ks = 0; ks < 6; ++ks) {
            const int st = nb * 6 + ks;
            SB();
            if (st + 3 < 12) ldstep(cb0, cb1, st + 3);
            else if (c < 9) ldstep(cn0, cn0, st + 3 - 12);
            SB();
#pragma unroll
            for (int q = 0; q < 3; ++q) acc[q] = mfma(af[st % 4], Of[ks][q], acc[q]);
          }
          SB();
#pragma unroll
          for (int q = 0; q < 3; ++q) pk[nb][q] = __builtin_convertvector(acc[q], bf16x4);
        }
#pragma unroll
        for (int q = 0; q < 3; ++q)
          __builtin_amdgcn_raw_buffer_store_b128(__builtin_bit_cast(u32x4, cat(pk[0][q], pk[1][q])), srdP, l16,
                                                 pbase + (unsigned)(c * 3 + q) * 1024u, 16  );
        if (c < 9) bar();
        cb0 = cn0; cb1 = cn0 ^ 32u;
      }
      waitvm<0>();
    }
    bar();
    const unsigned tag = E + 1u;
    if (hw == 0) {
      const unsigned l5 = lane_id();
      if (l5 == 0) __hip_atomic_store(epw + 32 * (1 + jj), tag, __ATOMIC_RELAXED, __HIP_MEMORY_SCOPE_AGENT);
      for (unsigned spin = 0; spin < (1u << 22); ++spin) {
        const unsigned v = __hip_atomic_load(epw + 32 * (1 + (l5 & 15)), __ATOMIC_RELAXED, __HIP_MEMORY_SCOPE_AGENT);
        if (__all(v == tag)) break;
        __builtin_amdgcn_s_sleep(1);
      }
      if (jj == 0 && l5 == 0) __hip_atomic_store(epw, tag, __ATOMIC_RELAXED, __HIP_MEMORY_SCOPE_AGENT);
      __builtin_amdgcn_fence(__ATOMIC_ACQUIRE, "wavefront");
    }
  }
  if (w < 4) bar();
  bar();
  STAMP(126);
  {
    const int piece = jj * 8 + w;
    const unsigned l6 = lane_id(), r6 = l6 & 15u, g6 = l6 >> 4;
    if (piece < 120) {
      float s[8];
#pragma unroll
      for (int i = 0; i < 8; ++i) s[i] = 0.f;
      u32x4 v[16];
#pragma unroll
      for (int k = 0; k < 16; ++k) {
        const unsigned obid = (unsigned)(((b * 2 + (k & 1)) << 3) | (k >> 1));
        v[k] = __builtin_amdgcn_raw_buffer_load_b128(srdP, l6 * 16u, obid * PART_BYTES + (unsigned)piece * 1024u, 16  );
      }
#pragma unroll
      for (int k = 0; k < 16; ++k)
#pragma unroll
        for (int i = 0; i < 4; ++i) { s[2 * i] += __uint_as_float(v[k][i] << 16); s[2 * i + 1] += __uint_as_float(v[k][i] & 0xffff0000u); }
      const int hp = piece / 30, rem = piece % 30, c = rem / 3, q = rem % 3;
      const int row = 48 * (hp & 1) + 16 * q + (int)r6, n0 = 64 * c + 32 * (hp >> 1) + 4 * (int)g6;
      __bf16* op = (__bf16*)outg + ((size_t)b * SQ + row) * DM + n0;
      *(bf16x4*)op = __builtin_convertvector((f32x4{s[0], s[1], s[2], s[3]}), bf16x4);
      *(bf16x4*)(op + 16) = __builtin_convertvector((f32x4{s[4], s[5], s[6], s[7]}), bf16x4);
    }
  }
  STAMP(127);
  STAMP_DUMP();
}

extern "C" void kernel_launch(void* const* d_in, const int* in_sizes, int n_in, void* d_out, int out_size, void* d_ws, size_t ws_size,
                              hipStream_t stream) {
  (void)in_sizes; (void)n_in; (void)out_size; (void)ws_size;
  (void)hipFuncSetAttribute((const void*)mha_kernel, hipFuncAttributeMaxDynamicSharedMemorySize, LDS_BYTES);
  mha_kernel<<<256, 512, LDS_BYTES, stream>>>(d_in[0], d_in[1], d_in[2], d_in[3], d_in[4], d_out, d_ws);
}
```

```cpp
#include <hip/hip_runtime.h>
#include <hip/hip_bf16.h>
#include <stdint.h>

typedef __bf16 bf16x8 __attribute__((ext_vector_type(8)));
typedef __bf16 bf16x4 __attribute__((ext_vector_type(4)));
typedef float f32x4 __attribute__((ext_vector_type(4)));
typedef float f32x2 __attribute__((ext_vector_type(2)));
typedef unsigned u32x4 __attribute__((ext_vector_type(4)));
typedef unsigned u32x2 __attribute__((ext_vector_type(2)));
typedef unsigned u32x10 __attribute__((ext_vector_type(10)));
#define AS3 __attribute__((address_space(3)))
#define AS1 __attribute__((address_space(1)))
#define DI __device__ __forceinline__
#define SB() __builtin_amdgcn_sched_barrier(0)

constexpr int NB = 16, SQ = 96, DM = 640, NH = 32, SK = 1024, DH = 192;
constexpr unsigned SLOT = 12288u, UNIT = 24576u;
constexpr unsigned WAREA = 3u * UNIT;
constexpr unsigned QBUF = 5u * UNIT;
constexpr unsigned MISC = QBUF + 36864u;
constexpr unsigned LDS_BYTES = 163840u;
constexpr unsigned KV_BYTES = (unsigned)NB * NH * SK * DH * 2u;
constexpr unsigned W_BYTES = (unsigned)DM * NH * DH * 2u;
constexpr unsigned X_BYTES = (unsigned)NB * SQ * DM * 2u;
constexpr unsigned PART_BYTES = 122880u;
constexpr unsigned WS_FLAGS = 0u;
constexpr unsigned WS_PART = 65536u;
constexpr int WARMC = 4;
constexpr int KV0 = 0, KV1 = 3, KV2 = 6;
constexpr unsigned CODE_WARM_BYTES = 30720u;
constexpr float SM_C = 0.07216878364870322f * 1.4426950408889634f;
constexpr float RESC_THR = 6.0f / SM_C;

DI void dma16(unsigned ldsaddr, unsigned voff, __amdgpu_buffer_rsrc_t srd, unsigned soff) {
  asm volatile("s_mov_b32 m0, %0\n\ts_nop 0\n\tbuffer_load_dwordx4 %1, %2, %3 offen lds" :: "s"(ldsaddr), "v"(voff), "s"(srd), "s"(soff) : "memory", "m0");
}
DI void dma16nt(unsigned ldsaddr, unsigned voff, __amdgpu_buffer_rsrc_t srd, unsigned soff) {
  asm volatile("s_mov_b32 m0, %0\n\ts_nop 0\n\tbuffer_load_dwordx4 %1, %2, %3 offen nt lds" :: "s"(ldsaddr), "v"(voff), "s"(srd), "s"(soff) : "memory", "m0");
}
template <int N> DI void waitvm() { asm volatile("s_waitcnt vmcnt(%0)" :: "i"(N) : "memory"); }
DI void wait_vm_dyn(int n) {
  if (n >= 19) waitvm<19>(); else if (n >= 14) waitvm<14>(); else if (n >= 12) waitvm<12>(); else if (n == 11) waitvm<11>(); else if (n >= 6) waitvm<6>(); else if (n == 5) waitvm<5>(); else waitvm<0>();
}
DI void bar() { asm volatile("s_waitcnt lgkmcnt(0)\n\ts_barrier" ::: "memory"); }
DI unsigned lane_id() { unsigned l; asm volatile("v_mbcnt_lo_u32_b32 %0, -1, 0\n\tv_mbcnt_hi_u32_b32 %0, -1, %0" : "=v"(l)); return l; }
DI unsigned rfl(unsigned x) { return __builtin_amdgcn_readfirstlane(x); }
DI bf16x8 ld128(unsigned a) { return *(const AS3 bf16x8*)(size_t)a; }
DI f32x4 ldf4(unsigned a) { return *(const AS3 f32x4*)(size_t)a; }
DI void st128(unsigned a, bf16x8 v) { *(AS3 bf16x8*)(size_t)a = v; }
DI void stu4(unsigned a, u32x4 v) { *(AS3 u32x4*)(size_t)a = v; }
DI void stf4(unsigned a, f32x4 v) { *(AS3 f32x4*)(size_t)a = v; }
DI bf16x4 ldtr(unsigned a) { return __builtin_amdgcn_ds_read_tr16_b64_v4bf16((AS3 bf16x4*)(size_t)a); }
DI bf16x8 cat(bf16x4 lo, bf16x4 hi) { return __builtin_shufflevector(lo, hi, 0, 1, 2, 3, 4, 5, 6, 7); }
DI f32x4 mfma(bf16x8 a, bf16x8 b, f32x4 c) { return __builtin_amdgcn_mfma_f32_16x16x32_bf16(a, b, c, 0, 0, 0); }
DI u32x4 bload(__amdgpu_buffer_rsrc_t srd, unsigned voff, unsigned soff) {
  return __builtin_amdgcn_raw_buffer_load_b128(srd, voff, soff, 0);
}
DI bf16x8 pack8(f32x4 a, f32x4 b) {
  bf16x4 lo = __builtin_convertvector(a, bf16x4), hi = __builtin_convertvector(b, bf16x4);
  return cat(lo, hi);
}
DI float max3f(float a, float b, float c) { float r; asm("v_max3_f32 %0, %1, %2, %3" : "=v"(r) : "v"(a), "v"(b), "v"(c)); return r; }
DI float max2f(float a, float b) { float r; asm("v_max_f32_e32 %0, %1, %2" : "=v"(r) : "v"(a), "v"(b)); return r; }
DI float xmax_g(float v) {
  auto a = __builtin_amdgcn_permlane16_swap(__float_as_uint(v), __float_as_uint(v), false, false);
  v = fmaxf(__uint_as_float(a[0]), __uint_as_float(a[1]));
  auto b = __builtin_amdgcn_permlane32_swap(__float_as_uint(v), __float_as_uint(v), false, false);
  return fmaxf(__uint_as_float(b[0]), __uint_as_float(b[1]));
}
DI float xsum_g(float v) {
  auto a = __builtin_amdgcn_permlane16_swap(__float_as_uint(v), __float_as_uint(v), false, false);
  v = __uint_as_float(a[0]) + __uint_as_float(a[1]);
  auto b = __builtin_amdgcn_permlane32_swap(__float_as_uint(v), __float_as_uint(v), false, false);
  return __uint_as_float(b[0]) + __uint_as_float(b[1]);
}

__global__ __launch_bounds__(512, 2) void mha_kernel(const void* __restrict__ xg, const void* __restrict__ kg, const void* __restrict__ vg,
                                                     const void* __restrict__ wqg, const void* __restrict__ wog, void* __restrict__ outg,
                                                     void* __restrict__ ws) {
  extern __shared__ __attribute__((aligned(16))) char lds_raw[];
  const unsigned ldsb = (unsigned)(size_t)(AS3 char*)lds_raw;
  const int tid = threadIdx.x, lane = tid & 63;
  const int w = __builtin_amdgcn_readfirstlane(tid >> 6);
  const int r = lane & 15, g = lane >> 4;
  const int bid = blockIdx.x, xcd = bid & 7, idx = bid >> 3;
  const int b = idx >> 1, jj = 2 * xcd + (idx & 1), h0 = 2 * jj;
  const unsigned kvbase = (unsigned)(b * NH + h0) * (unsigned)(SK * DH * 2);
  const int lw = w & 3;
  AS1 unsigned* const epw = (AS1 unsigned*)((char*)ws + WS_FLAGS) + (size_t)b * 17 * 32;
  const __amdgpu_buffer_rsrc_t srdP = __builtin_amdgcn_make_buffer_rsrc((char*)ws + WS_PART, 0, 256u * PART_BYTES, 0x00020000);
  const __amdgpu_buffer_rsrc_t srdW = __builtin_amdgcn_make_buffer_rsrc((void*)wqg, 0, W_BYTES, 0x00020000);
  const __amdgpu_buffer_rsrc_t srdX = __builtin_amdgcn_make_buffer_rsrc((void*)xg, 0, X_BYTES, 0x00020000);
  const __amdgpu_buffer_rsrc_t srdO = __builtin_amdgcn_make_buffer_rsrc((void*)wog, 0, W_BYTES, 0x00020000);
  const unsigned xbase = (unsigned)b * (unsigned)(SQ * DM * 2);
  auto obuf_addr = [&](int c) -> unsigned { const int k = c % 5; return k < 2 ? WAREA + (unsigned)k * UNIT : (unsigned)(k - 2) * UNIT; };

  if (w == 4 || w == 5) {
    const __amdgpu_buffer_rsrc_t srdC = __builtin_amdgcn_make_buffer_rsrc((void*)__builtin_amdgcn_s_getpc(), 0, CODE_WARM_BYTES, 0x00020000);
    dma16(rfl(ldsb + 2u * UNIT + 6144u * (unsigned)(w - 4)), lane * 16u, srdC, rfl((unsigned)idx * 2048u + (unsigned)(w - 4) * 1024u));
  }
  if (w < 4) {
    const int rh = w & 1, kh = w >> 1;
    {
      unsigned voW[3], voX[3];
#pragma unroll
      for (int i = 0; i < 3; ++i) {
        const unsigned p = 64u * (3u * lw + i) + lane;
        const unsigned row = p / 24u, c = p % 24u;
        voW[i] = row * 12288u + ((c ^ ((((row >> 1) & 1u) | (((row >> 3) & 1u) << 1)) << 1)) << 4);
        const unsigned rx = p >> 3, cx = p & 7u;
        voX[i] = rx * 1280u + ((cx ^ ((rx >> 1) & 7u)) << 4);
      }
      const unsigned stw = ldsb + 3072u * lw + lane * 16u;
      const unsigned wq0 = (unsigned)h0 * 384u;
      constexpr int QD = 4;
      u32x4 R[QD][9];
      auto ldc = [&](int s, int c) {
        const unsigned wo = (unsigned)(64 * c) * 12288u + wq0;
#pragma unroll
        for (int i = 0; i < 3; ++i) R[s][i] = bload(srdW, voW[i], wo);
#pragma unroll
        for (int i = 0; i < 3; ++i) R[s][3 + i] = bload(srdW, voW[i], wo + 32u * 12288u);
#pragma unroll
        for (int i = 0; i < 3; ++i) R[s][6 + i] = bload(srdX, voX[i], xbase + (unsigned)c * 128u);
      };
      auto stc = [&](int s, int c) {
        const unsigned base = stw + ((c & 1) ? WAREA : QBUF);
#pragma unroll
        for (int j = 0; j < 3; ++j)
#pragma unroll
          for (int i = 0; i < 3; ++i) stu4(base + (unsigned)j * SLOT + 1024u * i, R[s][3 * j + i]);
      };
      ldc(0, 0);
      ldc(1, 1);
      ldc(2, 2);
      u32x4 wd[4], we[8];
      SB();
      stc(0, 0);
      ldc(3, 3); ldc(0, 4);
      bar();
#pragma unroll
      for (int c = 0; c < 9; ++c) {
        stc((c + 1) % QD, c + 1);
        if (c + 1 + QD <= 9) ldc((c + 1) % QD, c + 1 + QD);
        if (c == WARMC) {
#pragma unroll
          for (int i = 0; i < 4; ++i) {
            const unsigned e = 60u * i + (lane < 60 ? lane : 59);
            const unsigned row = (unsigned)(40 * b + 10 * lw) + e / 24u;
            wd[i] = bload(srdW, row * 12288u + (e % 24u) * 16u, (unsigned)(h0 + 1) * 384u);
          }
          const unsigned so = (unsigned)(h0 * 192 + 24 * b) * 1280u + (unsigned)lw * 7680u;
#pragma unroll
          for (int i = 0; i < 8; ++i) we[i] = bload(srdO, (i < 7 || lane < 32 ? lane : 31) * 16u, so + 1024u * i);
        }
        bar();
      }
#pragma unroll
      for (int i = 0; i < 4; ++i) asm volatile("" :: "v"(wd[i]));
#pragma unroll
      for (int i = 0; i < 8; ++i) asm volatile("" :: "v"(we[i]));
    }
    const unsigned ln = lane_id();
    const unsigned r2 = ln & 15u, g2 = ln >> 4;
    const unsigned fk = (r2 >> 1) & 7;
    const unsigned ka0 = ldsb + kh * SLOT + 384u * r2 + ((g2 ^ fk) << 4);
    const unsigned ka1 = ldsb + kh * SLOT + 384u * r2 + (((4u + g2) ^ fk) << 4);
    const unsigned qa = ldsb + QBUF + (unsigned)rh * 18432u + ln * 16u;
    const unsigned qq = r2 >> 2, pp = r2 & 3;
    const unsigned fv = (2u * (g2 & 1) + (qq >> 1)) << 1;
    unsigned va[4];
#pragma unroll
    for (int d = 0; d < 4; ++d) va[d] = ldsb + kh * SLOT + 384u * (4u * g2 + qq) + (((2u * d + (pp >> 1)) ^ fv) << 4) + 8u * (pp & 1);

    bf16x8 Of1[6][3];
#pragma unroll 1
    for (int pr = 0; pr < 2; ++pr) {
      f32x4 O[12][3];
#pragma unroll
      for (int d = 0; d < 12; ++d)
#pragma unroll
        for (int q = 0; q < 3; ++q) O[d][q] = f32x4{0.f, 0.f, 0.f, 0.f};
      float mrun[3] = {-INFINITY, -INFINITY, -INFINITY};
      f32x2 lsum2[3] = {f32x2{0.f, 0.f}, f32x2{0.f, 0.f}, f32x2{0.f, 0.f}};
      unsigned kpos = pr == 0 ? 0u : 2u * UNIT;
      for (int t = 0; t < 16; ++t) {
        const unsigned vpos = kpos == 2u * UNIT ? 0u : kpos + UNIT;
        f32x4 S[2][3];
        {
          bf16x8 kf[3][2], qf[3][3];
          auto ldq = [&](int s) {
#pragma unroll
            for (int q = 0; q < 3; ++q) qf[s % 3][q] = ld128(qa + (unsigned)(q * 6 + s) * 1024u);
          };
          const unsigned k0 = ka0 + kpos, k1 = ka1 + kpos;
          auto ldk = [&](int s) {
            const unsigned kb = ((s & 1) ? k1 : k0) + 128u * (s >> 1);
            kf[s % 3][0] = ld128(kb); kf[s % 3][1] = ld128(kb + 6144u);
          };
          if (t > 0) { ldq(0); ldq(1); }
          asm volatile("s_barrier" ::: "memory");
          if (t == 0) { ldq(0); ldq(1); }
          ldk(0); ldk(1);
#pragma unroll
          for (int q = 0; q < 3; ++q) { S[0][q] = f32x4{0.f, 0.f, 0.f, 0.f}; S[1][q] = f32x4{0.f, 0.f, 0.f, 0.f}; }
#pragma unroll
          for (int s = 0; s < 6; ++s) {
            SB();
            if (s + 2 < 6) { ldk(s + 2); ldq(s + 2); }
            SB();
#pragma unroll
            for (int q = 0; q < 3; ++q) {
              S[0][q] = mfma(kf[s % 3][0], qf[s % 3][q], S[0][q]);
              S[1][q] = mfma(kf[s % 3][1], qf[s % 3][q], S[1][q]);
            }
          }
          SB();
        }
        bar();
        bf16x8 vf[7];
        auto ldv = [&](int d) {
          const unsigned a = va[d & 3] + vpos + 128u * (d >> 2);
          vf[d % 7] = cat(ldtr(a), ldtr(a + 6144u));
        };
        ldv(0); ldv(1); ldv(2); ldv(3); ldv(4); ldv(5);
        SB();
        float mx[3];
        bool need = false;
#pragma unroll
        for (int q = 0; q < 3; ++q) {
          mx[q] = max3f(max3f(S[0][q][0], S[0][q][1], S[0][q][2]), max3f(S[0][q][3], S[1][q][0], S[1][q][1]), max2f(S[1][q][2], S[1][q][3]));
          need |= (mx[q] > mrun[q] + RESC_THR);
        }
        if (__any(need)) {
#pragma unroll
          for (int q = 0; q < 3; ++q) {
            const float mn = fmaxf(mrun[q], xmax_g(mx[q]));
            const float al = __builtin_amdgcn_exp2f((mrun[q] - mn) * SM_C);
            mrun[q] = mn;
            lsum2[q] *= al;
#pragma unroll
            for (int d = 0; d < 12; ++d) O[d][q] *= al;
          }
        }
        bf16x8 pf[3];
#pragma unroll
        for (int q = 0; q < 3; ++q) {
          const float nm = -mrun[q] * SM_C;
          const f32x2 nm2 = {nm, nm}, c2 = {SM_C, SM_C};
          f32x2 e[4];
#pragma unroll
          for (int i = 0; i < 4; ++i) {
            const f32x2 sv = {S[i >> 1][q][2 * (i & 1)], S[i >> 1][q][2 * (i & 1) + 1]};
            const f32x2 a = sv * c2 + nm2;
            e[i] = f32x2{__builtin_amdgcn_exp2f(a[0]), __builtin_amdgcn_exp2f(a[1])};
          }
          lsum2[q] += (e[0] + e[1]) + (e[2] + e[3]);
          pf[q] = pack8(f32x4{e[0][0], e[0][1], e[1][0], e[1][1]}, f32x4{e[2][0], e[2][1], e[3][0], e[3][1]});
        }
#pragma unroll
        for (int d = 0; d < 12; ++d) {
          SB();
          if (d + 6 < 12) ldv(d + 6);
          SB();
#pragma unroll
          for (int q = 0; q < 3; ++q) O[d][q] = mfma(vf[d % 7], pf[q], O[d][q]);
        }
        SB();
        asm volatile("s_waitcnt lgkmcnt(0)" ::: "memory");
        kpos = vpos == 2u * UNIT ? 0u : vpos + UNIT;
      }
      float lsum[3];
      const unsigned lm = lane_id(), rm = lm & 15u;
      {
        const unsigned sa = ldsb + MISC + (unsigned)w * 384u + rm * 8u;
#pragma unroll
        for (int q = 0; q < 3; ++q) {
          lsum[q] = xsum_g(lsum2[q][0] + lsum2[q][1]);
          if (lm < 16) *(AS3 f32x2*)(size_t)(sa + q * 128u) = f32x2{mrun[q], lsum[q]};
        }
      }
      bar();
      float sc[3];
      {
        const unsigned sb = ldsb + MISC + (unsigned)(w ^ 2) * 384u + rm * 8u;
#pragma unroll
        for (int q = 0; q < 3; ++q) {
          const f32x2 o = *(const AS3 f32x2*)(size_t)(sb + q * 128u);
          const float mn = fmaxf(mrun[q], o[0]);
          const float a0 = __builtin_amdgcn_exp2f((mrun[q] - mn) * SM_C), a1 = __builtin_amdgcn_exp2f((o[0] - mn) * SM_C);
          const float l = lsum[q] * a0 + o[1] * a1;
          sc[q] = a0 / l;
        }
      }
      const unsigned xreg = pr == 0 ? WAREA : 0u;
      const unsigned lx = lane_id() * 16u;
      const unsigned xw = ldsb + xreg + (unsigned)w * 18432u + lx;
      const unsigned xr = ldsb + xreg + (unsigned)(w ^ 2) * 18432u + lx;
      const unsigned fw = pr == 0 ? xr : ldsb + QBUF + (unsigned)(w ^ 2) * 9216u + lx;
      if (kh) {
        asm volatile("" ::: "memory");
#pragma unroll
        for (int d = 0; d < 6; ++d)
#pragma unroll
          for (int q = 0; q < 3; ++q) { stf4(xw + (unsigned)(d * 3 + q) * 1024u, O[d][q] * sc[q]); O[d][q] = O[6 + d][q] * sc[q]; }
        asm volatile("" ::: "memory");
      } else {
#pragma unroll
        for (int d = 0; d < 6; ++d)
#pragma unroll
          for (int q = 0; q < 3; ++q) { stf4(xw + (unsigned)(d * 3 + q) * 1024u, O[6 + d][q] * sc[q]); O[d][q] = O[d][q] * sc[q]; }
      }
      bar();
#pragma unroll
      for (int d = 0; d < 6; ++d)
#pragma unroll
        for (int q = 0; q < 3; ++q) O[d][q] += ldf4(xr + (unsigned)(d * 3 + q) * 1024u);
      asm volatile("s_waitcnt lgkmcnt(0)" ::: "memory");
#pragma unroll
      for (int q = 0; q < 3; ++q)
#pragma unroll
        for (int s = 0; s < 3; ++s) st128(fw + (unsigned)(q * 3 + s) * 1024u, pack8(O[2 * s][q], O[2 * s + 1][q]));
      bar();
      if (pr == 0) bar();
    }
    {
      const unsigned lx = lane_id() * 16u;
#pragma unroll
      for (int k2 = 0; k2 < 2; ++k2) {
        const unsigned src = ldsb + QBUF + (unsigned)(rh + 2 * (1 - k2)) * 9216u + lx;
#pragma unroll
        for (int q = 0; q < 3; ++q)
#pragma unroll
          for (int s2 = 0; s2 < 3; ++s2) Of1[3 * k2 + s2][q] = ld128(src + (unsigned)(q * 3 + s2) * 1024u);
      }
      bar();
    }
    {
      const unsigned l4 = lane_id();
      const unsigned fo = ((l4 >> 4) & 3u) << 1;
      const unsigned voO = (l4 >> 3) * 1280u + (((l4 & 7u) ^ fo) << 4);
      const unsigned wob = (unsigned)(h0 + 1) * 192u * 1280u + (unsigned)lw * 48u * 1280u;
      auto issue = [&](int c) {
        const unsigned la = ldsb + obuf_addr(c) + (unsigned)lw * 6144u;
#pragma unroll
        for (int i = 0; i < 6; ++i) dma16(rfl(la + 1024u * i), voO, srdO, rfl(wob + (unsigned)i * 10240u + (unsigned)c * 128u));
      };
      const unsigned g4 = l4 >> 4, qq4 = (l4 & 15u) >> 2, pp4 = l4 & 3u;
      const unsigned fv4 = (2u * (g4 & 1) + (qq4 >> 1)) << 1;
      const unsigned oa1 = ldsb + 128u * (4u * g4 + qq4) + (((4u * kh + 2u + (pp4 >> 1)) ^ fv4) << 4) + 8u * (pp4 & 1);
      const unsigned exw = ldsb + QBUF + (unsigned)lw * 3072u + l4 * 16u;
      for (int c = 0; c < 10; ++c) {
        unsigned cb = oa1 + obuf_addr(c);
        asm volatile("" : "+v"(cb));
        bf16x8 af[6];
#pragma unroll
        for (int ks = 0; ks < 6; ++ks) af[ks] = cat(ldtr(cb + 4096u * ks), ldtr(cb + 4096u * ks + 2048u));
        SB();
        f32x4 acc[3] = {f32x4{0.f, 0.f, 0.f, 0.f}, f32x4{0.f, 0.f, 0.f, 0.f}, f32x4{0.f, 0.f, 0.f, 0.f}};
#pragma unroll
        for (int ks = 0; ks < 6; ++ks)
#pragma unroll
          for (int q = 0; q < 3; ++q) acc[q] = mfma(af[ks], Of1[ks][q], acc[q]);
#pragma unroll
        for (int q = 0; q < 3; ++q) stf4(exw + (unsigned)(c & 1) * 12288u + 1024u * q, acc[q]);
        SB();
        if (c >= 1 && c + 4 <= 9) issue(c + 4);
        SB();
        if (c <= 5) waitvm<18>(); else if (c == 6) waitvm<12>(); else if (c == 7) waitvm<6>(); else waitvm<0>();
        bar();
      }
    }
  } else {
    const int hw = w - 4, rh = hw & 1, xh = hw >> 1;
    __builtin_amdgcn_s_setprio(1);
    const __amdgpu_buffer_rsrc_t srdK = __builtin_amdgcn_make_buffer_rsrc((void*)kg, 0, KV_BYTES, 0x00020000);
    const __amdgpu_buffer_rsrc_t srdV = __builtin_amdgcn_make_buffer_rsrc((void*)vg, 0, KV_BYTES, 0x00020000);
    unsigned fpk = 0;
#pragma unroll
    for (int i = 0; i < 3; ++i) {
      const unsigned row = (64u * i + lane) / 24u;
      fpk |= ((row >> 1) & 7u) << (3 * i);
      fpk |= (((row >> 1) & 3u) << 1) << (16 + 3 * i);
    }
    const unsigned ldsw = ldsb + 6144u * hw;
    auto issueKV = [&](int u) {
      const unsigned pos = ldsw + (unsigned)(u % 3) * UNIT;
      const unsigned go = kvbase + (unsigned)(u >> 1) * UNIT;
      const unsigned l = lane_id();
      const unsigned base = 128u * (l >> 3) + 6144u * hw, c7 = l & 7u;
      if (u & 1) {
#pragma unroll
        for (int i = 0; i < 3; ++i) {
          const unsigned vo = base + ((c7 ^ ((fpk >> (16 + 3 * i)) & 7u)) << 4);
          dma16nt(rfl(pos + 1024u * i), vo, srdV, rfl(go + 1024u * i));
          dma16nt(rfl(pos + 3072u + 1024u * i), vo, srdV, rfl(go + 3072u + 1024u * i));
        }
      } else {
#pragma unroll
        for (int i = 0; i < 3; ++i) {
          const unsigned vo = base + ((c7 ^ ((fpk >> (3 * i)) & 7u)) << 4);
          dma16nt(rfl(pos + 1024u * i), vo, srdK, rfl(go + 1024u * i));
          dma16nt(rfl(pos + 3072u + 1024u * i), vo ^ 64u, srdK, rfl(go + 3072u + 1024u * i));
        }
      }
    };
    {
      const unsigned dst = rfl(ldsb + 2u * UNIT + 6144u * hw);
      const unsigned so = (unsigned)idx * 61440u + (unsigned)hw * 15360u;
#pragma unroll
      for (int i = 0; i < 15; ++i) dma16(dst, lane * 16u, srdX, rfl(so + 1024u * i));
#pragma unroll
      for (int i = 0; i < 4; ++i) {
        const unsigned e = 60u * i + (lane < 60 ? lane : 59);
        const unsigned row = (unsigned)(40 * b + 10 * hw) + e / 24u;
        dma16(dst, row * 12288u + (e % 24u) * 16u, srdW, rfl((unsigned)h0 * 384u));
      }
    }
    const unsigned qq = r >> 2, pp = r & 3;
    const unsigned fw = ((qq >> 1) | (((unsigned)g & 1u) << 1)) << 1;
    unsigned wa[6];
#pragma unroll
    for (int d = 0; d < 6; ++d) {
      const unsigned dg = 6u * xh + d;
      wa[d] = 384u * (8u * g + qq) + (((2u * (dg & 3u) + (pp >> 1)) ^ fw) << 4) + 8u * (pp & 1) + 128u * (dg >> 2);
    }
    f32x4 QA[6][3];
    auto zeroQA = [&]() {
#pragma unroll
      for (int d = 0; d < 6; ++d)
#pragma unroll
        for (int q = 0; q < 3; ++q) QA[d][q] = f32x4{0.f, 0.f, 0.f, 0.f};
    };
    auto writeQ = [&]() {
      const unsigned l = lane_id(), rr = l & 15u, gg = l >> 4;
      const unsigned qw = ldsb + QBUF + (unsigned)rh * 18432u + (rr + 16u * (gg >> 1)) * 16u + 8u * (gg & 1u) + (unsigned)xh * 3072u;
#pragma unroll
      for (int q = 0; q < 3; ++q)
#pragma unroll
        for (int d = 0; d < 6; ++d)
          *(AS3 bf16x4*)(size_t)(qw + (unsigned)(q * 6 + (d >> 1)) * 1024u + 512u * (d & 1)) = __builtin_convertvector(QA[d][q], bf16x4);
    };
    auto qload = [&](unsigned wb, bf16x8 (&af)[6]) {
#pragma unroll
      for (int i = 0; i < 6; ++i) { const unsigned ad = wb + wa[i]; af[i] = cat(ldtr(ad), ldtr(ad + 1536u)); }
    };
    auto qmma = [&](const bf16x8 (&af)[6], const bf16x8 (&xf)[3]) {
#pragma unroll
      for (int i = 0; i < 6; ++i)
#pragma unroll
        for (int q = 0; q < 3; ++q) QA[i][q] = mfma(af[i], xf[q], QA[i][q]);
    };
    zeroQA();
    {
      const unsigned fk = (r >> 1) & 7;
      const unsigned xa0 = 128u * (48u * rh + r) + (((unsigned)g ^ fk) << 4);
      const unsigned xa1 = 128u * (48u * rh + r) + (((4u + g) ^ fk) << 4);
      for (int c = 0; c < 10; ++c) {
        bar();
        if (c == KV0) issueKV(0); else if (c == KV1) issueKV(1); else if (c == KV2) issueKV(2);
        const unsigned base = ldsb + ((c & 1) ? WAREA : QBUF);
        bf16x8 xf0[3], xf1[3], af0[6], af1[6];
#pragma unroll
        for (int q = 0; q < 3; ++q) xf0[q] = ld128(base + 2u * SLOT + xa0 + 2048u * q);
        qload(base, af0);
        SB();
#pragma unroll
        for (int q = 0; q < 3; ++q) xf1[q] = ld128(base + 2u * SLOT + xa1 + 2048u * q);
#pragma unroll
        for (int i = 0; i < 6; ++i) {
          SB();
          { const unsigned ad = base + SLOT + wa[i]; af1[i] = cat(ldtr(ad), ldtr(ad + 1536u)); }
          SB();
#pragma unroll
          for (int q = 0; q < 3; ++q) QA[i][q] = mfma(af0[i], xf0[q], QA[i][q]);
        }
        SB();
        qmma(af1, xf1);
        SB();
      }
    }
    writeQ();
    zeroQA();
    {
      unsigned voW[3], voX0, voX1;
#pragma unroll
      for (int i = 0; i < 3; ++i) {
        const unsigned p = 64u * (3u * hw + i) + lane;
        const unsigned row = p / 24u, c = p % 24u;
        voW[i] = row * 12288u + ((c ^ ((((row >> 1) & 1u) | (((row >> 3) & 1u) << 1)) << 1)) << 4);
      }
      {
        const unsigned p0 = 96u * hw + lane, p1 = p0 + 64u;
        voX0 = (p0 >> 2) * 1280u + (((p0 & 3u) ^ ((0u - (p0 >> 4)) & 3u)) << 4);
        voX1 = (p1 >> 2) * 1280u + (((p1 & 3u) ^ ((0u - (p1 >> 4)) & 3u)) << 4);
      }
      const unsigned xra = 64u * (48u * rh + r) + ((((unsigned)g) ^ ((0u - ((unsigned)r >> 2)) & 3u)) << 4);
      const unsigned wq1 = (unsigned)(h0 + 1) * 384u;
      int prev = 12;
      int jn = 0, jc = 0;
      for (int h = 0; h < 32; ++h) {
        wait_vm_dyn(prev);
        bar();
        int cnt = 0;
        const int hm = h % 3;
        const bool docomp = (h >= 2 && hm != 1 && jc < 20);
        bf16x8 xf[3], af[6];
        if (docomp) {
          const unsigned wbuf = ldsb + WAREA + (unsigned)(jc & 1) * 18432u;
#pragma unroll
          for (int q = 0; q < 3; ++q) xf[q] = ld128(wbuf + SLOT + xra + 1024u * q);
          qload(wbuf, af);
        }
        SB();
        if (h >= 1) { issueKV(h + 2); }
        cnt += 6;
        if (hm != 2 && jn < 20) {
          const unsigned wbuf = ldsb + WAREA + (unsigned)(jn & 1) * 18432u;
          const unsigned so = (unsigned)(32 * jn) * 12288u + wq1;
#pragma unroll
          for (int i = 0; i < 3; ++i) dma16(rfl(wbuf + 3072u * hw + 1024u * i), voW[i], srdW, rfl(so));
          const unsigned xo = xbase + (unsigned)jn * 64u;
          dma16(rfl(wbuf + SLOT + 1536u * hw), voX0, srdX, rfl(xo));
          if (lane < 32) dma16(rfl(wbuf + SLOT + 1536u * hw + 1024u), voX1, srdX, rfl(xo));
          ++jn; cnt += 5;
        }
        SB();
        if (docomp) { qmma(af, xf); ++jc; }
        SB();
        prev = cnt;
      }
    }
    bar();
    issueKV(34);
    bar(); bar();
    bf16x8 Of[6][3];
    auto readO = [&](unsigned xreg, unsigned stride) {
#pragma unroll
      for (int k2 = 0; k2 < 2; ++k2) {
        const unsigned src = ldsb + xreg + (unsigned)(rh + 2 * (1 - k2)) * stride + lane_id() * 16u;
#pragma unroll
        for (int q = 0; q < 3; ++q)
#pragma unroll
          for (int s = 0; s < 3; ++s) Of[3 * k2 + s][q] = ld128(src + (unsigned)(q * 3 + s) * 1024u);
      }
    };
    readO(WAREA, 18432u);
    bar();
    writeQ();
    const unsigned l3 = lane_id(), g3 = l3 >> 4, qq3 = (l3 & 15u) >> 2, pp3 = l3 & 3u;
    const unsigned fv = (2u * (g3 & 1) + (qq3 >> 1)) << 1;
    const unsigned oa0 = 128u * (4u * g3 + qq3) + (((4u * xh + (pp3 >> 1)) ^ fv) << 4) + 8u * (pp3 & 1);
    const unsigned fo = ((l3 >> 4) & 3u) << 1;
    const unsigned voO = (l3 >> 3) * 1280u + (((l3 & 7u) ^ fo) << 4);
    u32x10 PP[2][3][2];
#pragma unroll
    for (int nb = 0; nb < 2; ++nb)
#pragma unroll
      for (int q = 0; q < 3; ++q)
#pragma unroll
        for (int d = 0; d < 2; ++d)
#pragma unroll
          for (int c = 0; c < 10; ++c) PP[nb][q][d][c] = 0u;
    {
      const unsigned wob0 = (unsigned)h0 * 192u * 1280u + (unsigned)hw * 48u * 1280u;
      auto half = [&](int h, int nb, int cc, bool comp, int ci, int prev) -> int {
        wait_vm_dyn(prev);
        bar();
        bf16x8 af[4];
        unsigned oab = (oa0 ^ (32u * nb)) + ldsb + WAREA + (unsigned)(cc & 1) * UNIT;
        asm volatile("" : "+v"(oab));
        auto ldo = [&](int ks) { af[ks & 3] = cat(ldtr(oab + 4096u * ks), ldtr(oab + 4096u * ks + 2048u)); };
        if (comp) { ldo(0); ldo(1); ldo(2); ldo(3); }
        SB();
        int cnt = 0;
        if (h >= 1 && 34 + h < 64) issueKV(34 + h);
        if (34 + h < 64) cnt += 6;
        if (ci >= 0) {
          const unsigned wob = wob0 + (ci >= 10 ? 192u * 1280u + (unsigned)(ci - 10) * 128u : (unsigned)ci * 128u);
#pragma unroll
          for (int i = 0; i < 6; ++i) dma16(rfl(ldsb + WAREA + (unsigned)(ci & 1) * UNIT + 6144u * hw + 1024u * i), voO, srdO, rfl(wob + (unsigned)i * 10240u));
          cnt += 6;
        }
        SB();
        if (comp) {
          f32x4 acc[3] = {f32x4{0.f, 0.f, 0.f, 0.f}, f32x4{0.f, 0.f, 0.f, 0.f}, f32x4{0.f, 0.f, 0.f, 0.f}};
#pragma unroll
          for (int ks = 0; ks < 6; ++ks) {
#pragma unroll
            for (int q = 0; q < 3; ++q) acc[q] = mfma(af[ks & 3], Of[ks][q], acc[q]);
            SB();
            if (ks + 4 < 6) ldo(ks + 4);
            SB();
          }
#pragma unroll
          for (int q = 0; q < 3; ++q) {
            const u32x2 pk = __builtin_bit_cast(u32x2, __builtin_convertvector(acc[q], bf16x4));
            PP[nb][q][0][cc] = pk[0]; PP[nb][q][1][cc] = pk[1];
          }
        }
        SB();
        return cnt;
      };
      int prev = 12;
      for (int i = 0; i < 12; ++i) {
        prev = half(2 * i, 1, i - 2, i >= 2, -1, prev);
        prev = half(2 * i + 1, 0, i - 1, i >= 1 && i <= 10, i, prev);
      }
      {
        u32x4 FR[3][6];
        const unsigned wob1 = wob0 + 192u * 1280u;
#pragma unroll
        for (int i = 12; i < 16; ++i) {
#pragma unroll
          for (int hh = 0; hh < 2; ++hh) {
            const int h = 2 * i + hh;
            wait_vm_dyn(prev);
            bar();
            int cnt = 0;
            if (34 + h < 64) { issueKV(34 + h); cnt += 6; }
            if (hh == 1 && i < 15) {
#pragma unroll
              for (int k = 0; k < 6; ++k) FR[i - 12][k] = bload(srdO, voO, wob1 + (unsigned)k * 10240u + (unsigned)(i - 10) * 128u);
              cnt += 6;
            }
            SB();
            prev = cnt;
          }
        }
        waitvm<0>();
        bar(); bar(); bar();
        const unsigned fd = ldsb + (unsigned)hw * 6144u + lane_id() * 16u;
#pragma unroll
        for (int c = 0; c < 3; ++c)
#pragma unroll
          for (int k = 0; k < 6; ++k) stu4(fd + (unsigned)c * UNIT + 1024u * k, FR[c][k]);
      }
    }
    readO(QBUF, 9216u);
    bar();
    {
      const unsigned pbase = (unsigned)bid * PART_BYTES + (unsigned)hw * 30720u;
      const unsigned l16 = lane_id() * 16u;
      const unsigned exr = ldsb + QBUF + (unsigned)hw * 3072u + l16;
      bf16x4 pk0[3];
      for (int c = 0; c <= 10; ++c) {
        bf16x8 af[4];
        unsigned cb = oa0 + ldsb + obuf_addr(c);
        asm volatile("" : "+v"(cb));
        auto ldo = [&](int ks) { af[ks & 3] = cat(ldtr(cb + 4096u * ks), ldtr(cb + 4096u * ks + 2048u)); };
        if (c < 10) { ldo(0); ldo(1); ldo(2); ldo(3); }
        SB();
        if (c >= 1) {
          f32x4 ex[3];
#pragma unroll
          for (int q = 0; q < 3; ++q) ex[q] = ldf4(exr + (unsigned)((c - 1) & 1) * 12288u + 1024u * q);
#pragma unroll
          for (int q = 0; q < 3; ++q) {
            const unsigned u0 = PP[1][q][0][c - 1], u1 = PP[1][q][1][c - 1];
            const f32x4 p = f32x4{__uint_as_float(u0 << 16), __uint_as_float(u0 & 0xffff0000u), __uint_as_float(u1 << 16), __uint_as_float(u1 & 0xffff0000u)};
            __builtin_amdgcn_raw_buffer_store_b128(__builtin_bit_cast(u32x4, cat(pk0[q], __builtin_convertvector(ex[q] + p, bf16x4))), srdP, l16,
                                                   pbase + (unsigned)((c - 1) * 3 + q) * 1024u, 16  );
          }
        }
        SB();
        if (c < 10) {
          f32x4 acc[3];
#pragma unroll
          for (int q = 0; q < 3; ++q) {
            const unsigned u0 = PP[0][q][0][c], u1 = PP[0][q][1][c];
            acc[q] = f32x4{__uint_as_float(u0 << 16), __uint_as_float(u0 & 0xffff0000u), __uint_as_float(u1 << 16), __uint_as_float(u1 & 0xffff0000u)};
          }
#pragma unroll
          for (int ks = 0; ks < 6; ++ks) {
#pragma unroll
            for (int q = 0; q < 3; ++q) acc[q] = mfma(af[ks & 3], Of[ks][q], acc[q]);
            SB();
            if (ks + 4 < 6) ldo(ks + 4);
            SB();
          }
#pragma unroll
          for (int q = 0; q < 3; ++q) pk0[q] = __builtin_convertvector(acc[q], bf16x4);
          bar();
        }
      }
    }
    unsigned Ev = 0u;
    if (hw == 0) Ev = __hip_atomic_load(epw, __ATOMIC_RELAXED, __HIP_MEMORY_SCOPE_AGENT);
    waitvm<0>();
    bar();
    if (hw == 0) {
      const unsigned tag = rfl(Ev) + 1u;
      const unsigned l5 = lane_id();
      if (l5 == 0) __hip_atomic_store(epw + 32 * (1 + jj), tag, __ATOMIC_RELAXED, __HIP_MEMORY_SCOPE_AGENT);
      for (unsigned spin = 0; spin < (1u << 22); ++spin) {
        const unsigned v = __hip_atomic_load(epw + 32 * (1 + (l5 & 15)), __ATOMIC_RELAXED, __HIP_MEMORY_SCOPE_AGENT);
        if (__all(v == tag)) break;
        __builtin_amdgcn_s_sleep(1);
      }
      if (jj == 0 && l5 == 0) __hip_atomic_store(epw, tag, __ATOMIC_RELAXED, __HIP_MEMORY_SCOPE_AGENT);
      __builtin_amdgcn_fence(__ATOMIC_ACQUIRE, "wavefront");
    }
  }
  if (w < 4) bar();
  bar();
  {
    const int piece = jj * 8 + w;
    const unsigned l6 = lane_id(), r6 = l6 & 15u, g6 = l6 >> 4;
    if (piece < 120) {
      float s[8];
#pragma unroll
      for (int i = 0; i < 8; ++i) s[i] = 0.f;
      u32x4 v[16];
#pragma unroll
      for (int k = 0; k < 16; ++k) {
        const unsigned obid = (unsigned)(((b * 2 + (k & 1)) << 3) | (k >> 1));
        v[k] = __builtin_amdgcn_raw_buffer_load_b128(srdP, l6 * 16u, obid * PART_BYTES + (unsigned)piece * 1024u, 16  );
      }
#pragma unroll
      for (int k = 0; k < 16; ++k)
#pragma unroll
        for (int i = 0; i < 4; ++i) { s[2 * i] += __uint_as_float(v[k][i] << 16); s[2 * i + 1] += __uint_as_float(v[k][i] & 0xffff0000u); }
      const int hp = piece / 30, rem = piece % 30, c = rem / 3, q = rem % 3;
      const int row = 48 * (hp & 1) + 16 * q + (int)r6, n0 = 64 * c + 32 * (hp >> 1) + 4 * (int)g6;
      __bf16* op = (__bf16*)outg + ((size_t)b * SQ + row) * DM + n0;
      *(bf16x4*)op = __builtin_convertvector((f32x4{s[0], s[1], s[2], s[3]}), bf16x4);
      *(bf16x4*)(op + 16) = __builtin_convertvector((f32x4{s[4], s[5], s[6], s[7]}), bf16x4);
    }
  }
}

extern "C" void kernel_launch(void* const* d_in, const int* in_sizes, int n_in, void* d_out, int out_size, void* d_ws, size_t ws_size,
                              hipStream_t stream) {
  (void)in_sizes; (void)n_in; (void)out_size; (void)ws_size;
  (void)hipFuncSetAttribute((const void*)mha_kernel, hipFuncAttributeMaxDynamicSharedMemorySize, LDS_BYTES);
  mha_kernel<<<256, 512, LDS_BYTES, stream>>>(d_in[0], d_in[1], d_in[2], d_in[3], d_in[4], d_out, d_ws);
}
```
